```python
import torch

def _softmax(x, axis=-1):
    return torch.softmax(x, dim=axis)

def _gelu(x):
    return torch.nn.functional.gelu(x, approximate='tanh')

def _silu(x):
    return torch.nn.functional.silu(x)

def _sigmoid(x):
    return torch.sigmoid(x)

def _layernorm(x, axis=-1, eps=1e-05):
    m = x.mean(dim=axis, keepdim=True)
    v = x.var(dim=axis, keepdim=True, unbiased=False)
    return (x - m) / torch.sqrt(v + eps)

def _scatter_add(tgt, idx, src, axis=0):
    return tgt.clone().index_add_(axis, idx, src)

def _segment_sum(src, seg, n):
    out = torch.zeros((n,) + src.shape[1:], dtype=src.dtype)
    return out.index_add_(0, seg, src)

def _pad(x, pw):
    flat = tuple((v for pair in reversed(pw) for v in pair))
    return torch.nn.functional.pad(x, flat)
kernel_name = 'sequential_preLN__memory_aug_recurrent_mem_tr__glu_gated_bilinear__prompt'

def setup_inputs():

    _w = lambda a: (lambda t: t.to(torch.bfloat16) if t.is_floating_point() else t)(torch.as_tensor(a))

    def _A_setup():
        B, S, Dm = (1, 2048, 640)
        H, Dh = (32, 192)
        M, seg = (8, 32)
        seg = min(seg, S)
        S = S // seg * seg
        return {'x': _w((lambda *s: torch.randn(*s, dtype=torch.bfloat16) * 0.05)(B, S, Dm)), 'mem0': _w((lambda *s: torch.randn(*s, dtype=torch.bfloat16) * 0.2)(B, M, Dm)), 'Wq': _w((lambda *s: torch.randn(*s, dtype=torch.bfloat16) * 1.6)(Dm, H, Dh) * Dm ** (-0.5)), 'Wk': _w((lambda *s: torch.randn(*s, dtype=torch.bfloat16) * 1.6)(Dm, H, Dh) * Dm ** (-0.5)), 'Wv': _w((lambda *s: torch.randn(*s, dtype=torch.bfloat16) * 0.2)(Dm, H, Dh) * Dm ** (-0.5)), 'Wo': _w((lambda *s: torch.randn(*s, dtype=torch.bfloat16) * 0.2)(H, Dh, Dm) * (H * Dh) ** (-0.5)), 'seg_len': seg}
    _A_in = _A_setup()

    def _B_setup():
        B, S, D, F = (1, 2048, 640, 1536)
        return {'x': _w((lambda *s: torch.randn(*s, dtype=torch.bfloat16) * 0.05)(B, S, D)), 'w_gate': _w((lambda *s: torch.randn(*s, dtype=torch.bfloat16) * 0.2)(D, F) * D ** (-0.5)), 'w_up': _w((lambda *s: torch.randn(*s, dtype=torch.bfloat16) * 0.2)(D, F) * D ** (-0.5)), 'w_down': _w((lambda *s: torch.randn(*s, dtype=torch.bfloat16) * 0.2)(F, D) * F ** (-0.5))}
    _B_in = _B_setup()
    return {'A_x': _A_in['x'], 'A_mem0': _A_in['mem0'], 'A_Wq': _A_in['Wq'], 'A_Wk': _A_in['Wk'], 'A_Wv': _A_in['Wv'], 'A_Wo': _A_in['Wo'], 'A_seg_len': _A_in['seg_len'], 'B_x': _B_in['x'], 'B_w_gate': _B_in['w_gate'], 'B_w_up': _B_in['w_up'], 'B_w_down': _B_in['w_down']}

def reference(A_x, A_mem0, A_Wq, A_Wk, A_Wv, A_Wo, A_seg_len, B_x, B_w_gate, B_w_up, B_w_down):

    def _A_ref(x, mem0, Wq, Wk, Wv, Wo, seg_len):
        B, S, Dm = x.shape
        H, Dh = (Wq.shape[1], Wq.shape[2])
        M = mem0.shape[1]
        scale = Dh ** (-0.5)
        n_seg = S // seg_len
        L = M + seg_len
        idx = torch.arange(L)
        mask = (idx[:, None] < M) | (idx[:, None] >= idx[None, :])
        mem = mem0
        outs = []
        for s in range(n_seg):
            xs = x[:, s * seg_len:(s + 1) * seg_len, :].clone()
            h = torch.concatenate([mem, xs], axis=1)
            q = torch.einsum('btd,dhe->bhte', h, Wq)
            k = torch.einsum('btd,dhe->bhte', h, Wk)
            v = torch.einsum('btd,dhe->bhte', h, Wv)
            scores = torch.einsum('bhqe,bhke->bhqk', q, k) * scale
            scores = torch.where(mask[None, None], scores, -1000000000.0)
            p = _softmax(scores, axis=-1)
            o = torch.einsum('bhqk,bhke->bhqe', p, v)
            o = torch.einsum('bhte,hed->btd', o, Wo)
            mem = o[:, :M, :].clone()
            outs.append(o[:, M:, :])
        return torch.concatenate(outs, axis=1)

    def _B_ref(x, w_gate, w_up, w_down):
        g = torch.einsum('bsd,df->bsf', x, w_gate)
        u = torch.einsum('bsd,df->bsf', x, w_up)
        h = g * u
        return torch.einsum('bsf,fd->bsd', h, w_down)
    x = A_x
    h = x + _A_ref(_layernorm(x, axis=-1), A_mem0, A_Wq, A_Wk, A_Wv, A_Wo, A_seg_len)
    h2 = h + _B_ref(_layernorm(h, axis=-1), B_w_gate, B_w_up, B_w_down)
    return h2
```

```cpp
#include <hip/hip_runtime.h>
#include <hip/hip_bf16.h>
#include <stdint.h>

typedef unsigned short u16;
typedef unsigned int u32;
typedef unsigned long long u64;
typedef short bf16x8 __attribute__((ext_vector_type(8)));
typedef short bf16x4 __attribute__((ext_vector_type(4)));
typedef float f32x4 __attribute__((ext_vector_type(4)));
typedef u32 u32x4 __attribute__((ext_vector_type(4)));
typedef u32 u32x2 __attribute__((ext_vector_type(2)));
#define LAS __attribute__((address_space(3)))

constexpr int S_ = 2048, D_ = 640, H_ = 32, E_ = 192, HE_ = H_ * E_;
constexpr int M_ = 8, SEG_ = 32, NSEG_ = 64, L_ = 40, F_ = 1536;

constexpr size_t WS_CTL   = 0;
constexpr size_t WS_GA2   = 4096;
constexpr size_t WS_GB    = WS_GA2 + 2560 * 8;
constexpr size_t WS_GA1   = WS_GB + (size_t)128 * 512 * 8;
constexpr size_t WS_ZEND  = WS_GA1 + (size_t)128 * 32 * 40 * 8;
constexpr size_t WS_XN    = (WS_ZEND + 4095) / 4096 * 4096;
constexpr size_t WS_QX    = WS_XN + (size_t)S_ * D_ * 2;
constexpr size_t WS_KX    = WS_QX + (size_t)S_ * HE_ * 2;
constexpr size_t WS_VX    = WS_KX + (size_t)S_ * HE_ * 2;
constexpr size_t WS_KM    = WS_VX + (size_t)S_ * HE_ * 2;
constexpr size_t WS_VM    = WS_KM + (size_t)NSEG_ * M_ * HE_ * 2;
constexpr size_t WS_OX    = WS_VM + (size_t)NSEG_ * M_ * HE_ * 2;
constexpr size_t WS_H     = WS_OX + (size_t)S_ * HE_ * 2;
constexpr size_t WS_HN    = WS_H + (size_t)S_ * D_ * 2;
constexpr size_t WS_GU    = WS_HN + (size_t)S_ * D_ * 2;
constexpr size_t WS_END   = WS_GU + (size_t)S_ * F_ * 2;

__device__ __forceinline__ float bf2f(u16 v) { return __uint_as_float(((u32)v) << 16); }
__device__ __forceinline__ u16 f2bf(float f) {
  __hip_bfloat16 b = __float2bfloat16(f);
  return *reinterpret_cast<u16*>(&b);
}
__device__ __forceinline__ float rbf(float f) { return bf2f(f2bf(f)); }
__device__ __forceinline__ u32 pack2(float a, float b) { return (u32)f2bf(a) | ((u32)f2bf(b) << 16); }

__device__ __forceinline__ f32x4 mfma16(bf16x8 a, bf16x8 b, f32x4 c) {
  return __builtin_amdgcn_mfma_f32_16x16x32_bf16(a, b, c, 0, 0, 0);
}
__device__ __forceinline__ bf16x4 tr_read(const void* p) {
  return __builtin_amdgcn_ds_read_tr16_b64_v4i16((LAS bf16x4*)p);
}
__device__ __forceinline__ bf16x8 cat8(bf16x4 a, bf16x4 b) {
  bf16x8 r; r[0]=a[0]; r[1]=a[1]; r[2]=a[2]; r[3]=a[3]; r[4]=b[0]; r[5]=b[1]; r[6]=b[2]; r[7]=b[3]; return r;
}

__global__ __launch_bounds__(256) void k_ln(const u16* __restrict__ x, u16* __restrict__ y, int rows) {
  int row = blockIdx.x * 4 + (threadIdx.x >> 6);
  int lane = threadIdx.x & 63;
  if (row >= rows) return;
  const u32* xr = (const u32*)(x + (size_t)row * D_);
  float v[10];
  float sum = 0.f;
#pragma unroll
  for (int i = 0; i < 5; ++i) {
    u32 w = xr[i * 64 + lane];
    v[2 * i] = bf2f((u16)(w & 0xffff)); v[2 * i + 1] = bf2f((u16)(w >> 16));
    sum += v[2 * i] + v[2 * i + 1];
  }
#pragma unroll
  for (int m = 32; m >= 1; m >>= 1) sum += __shfl_xor(sum, m);
  float mean = sum * (1.0f / D_);
  float sq = 0.f;
#pragma unroll
  for (int i = 0; i < 10; ++i) { float d = v[i] - mean; sq += d * d; }
#pragma unroll
  for (int m = 32; m >= 1; m >>= 1) sq += __shfl_xor(sq, m);
  float var = sq * (1.0f / D_);
  float mb = rbf(mean), vb = rbf(var);
  float den = rbf(sqrtf(rbf(vb + 1e-5f)));
  u32* yr = (u32*)(y + (size_t)row * D_);
#pragma unroll
  for (int i = 0; i < 5; ++i) {
    float a = rbf(v[2 * i] - mb) / den, b = rbf(v[2 * i + 1] - mb) / den;
    yr[i * 64 + lane] = pack2(a, b);
  }
}

enum { EPI_STORE = 0, EPI_RESID = 1, EPI_GLU = 2 };
struct GemmP {
  const u16* A; const u16* B0; const u16* B1; const u16* B2;
  u16* C0; u16* C1; u16* C2; const u16* R;
  int lda, ldb, ldc, K, nPerMat, pad0;
};

template <int BN, int EPI>
__device__ __forceinline__ void gemm_tile(const GemmP& p, int m0, int n0, char* smem) {
  constexpr int BM = 256, BK = 64;
  constexpr int A_BYTES = BM * BK * 2;
  constexpr int B_BYTES = BK * BN * 2;
  constexpr int ST_BYTES = A_BYTES + B_BYTES;
  constexpr int NT = BN / 32;
  constexpr int BROW = BN * 2;
  constexpr int BCH = BN / 8;
  const int tid = threadIdx.x, wave = tid >> 6, lane = tid & 63;
  const int wm = wave >> 1, wn = wave & 1;
  const int fr = lane & 15, g = lane >> 4;

  const u16* Bp; u16* Cp; int nb;
  if (EPI == EPI_GLU) { Bp = p.B0; Cp = p.C0; nb = n0; }
  else {
    int mat = n0 / p.nPerMat; nb = n0 - mat * p.nPerMat;
    Bp = mat == 0 ? p.B0 : (mat == 1 ? p.B1 : p.B2);
    Cp = mat == 0 ? p.C0 : (mat == 1 ? p.C1 : p.C2);
  }
  const u16* asrc[4];
#pragma unroll
  for (int i = 0; i < 4; ++i) {
    int b = (i * 512 + tid) * 16; int row = b >> 7; int pos = (b >> 4) & 7; int ch = pos ^ (row & 7);
    asrc[i] = p.A + (size_t)(m0 + row) * p.lda + ch * 8;
  }
  constexpr int NBI = B_BYTES / (512 * 16);
  const u16* bsrc[NBI];
#pragma unroll
  for (int i = 0; i < NBI; ++i) {
    int b = (i * 512 + tid) * 16; int row = b / BROW; int pos = (b % BROW) >> 4;
    int ch = pos ^ (((row & 3) << 2) | ((row >> 2) & 3));
    if (EPI == EPI_GLU) {
      int w2 = ch / (BCH / 2), within = ch % (BCH / 2); int isup = within / (BCH / 4); int c = within % (BCH / 4);
      const u16* base = isup ? p.B1 : p.B0;
      bsrc[i] = base + (size_t)row * p.ldb + nb + w2 * (BN / 4) + c * 8;
    } else {
      bsrc[i] = Bp + (size_t)row * p.ldb + nb + ch * 8;
    }
  }
  auto stage = [&](int kt, int buf) {
    char* sa = smem + buf * ST_BYTES; char* sb = sa + A_BYTES;
#pragma unroll
    for (int i = 0; i < 4; ++i)
      __builtin_amdgcn_global_load_lds((const u32*)(asrc[i] + kt * BK), (LAS u32*)(sa + (i * 512 + wave * 64) * 16), 16, 0, 0);
#pragma unroll
    for (int i = 0; i < NBI; ++i)
      __builtin_amdgcn_global_load_lds((const u32*)(bsrc[i] + (size_t)kt * BK * p.ldb), (LAS u32*)(sb + (i * 512 + wave * 64) * 16), 16, 0, 0);
  };

  f32x4 acc[4][NT];
#pragma unroll
  for (int a = 0; a < 4; ++a)
#pragma unroll
    for (int b = 0; b < NT; ++b) acc[a][b] = (f32x4){0.f, 0.f, 0.f, 0.f};

  const int nk = p.K / BK;
  stage(0, 0);
  for (int kt = 0; kt < nk; ++kt) {
    asm volatile("s_waitcnt vmcnt(0)" ::: "memory");
    __builtin_amdgcn_s_barrier();
    asm volatile("" ::: "memory");
    if (kt + 1 < nk) stage(kt + 1, (kt + 1) & 1);
    const char* sa = smem + (kt & 1) * ST_BYTES; const char* sb = sa + A_BYTES;
#pragma unroll
    for (int kk = 0; kk < 2; ++kk) {
      bf16x8 af[4], bfr[NT];
#pragma unroll
      for (int a = 0; a < 4; ++a) {
        int row = wm * 64 + a * 16 + fr; int ch = kk * 4 + g;
        af[a] = *(const bf16x8*)(sa + row * 128 + ((ch ^ (row & 7)) << 4));
      }
      const int q = fr >> 2, pp = fr & 3;
#pragma unroll
      for (int b = 0; b < NT; ++b) {
        int ch = (wn * (BN / 2) + b * 16) / 8 + (pp >> 1);
        int r0 = kk * 32 + g * 8 + q, r1 = r0 + 4;
        bf16x4 lo = tr_read(sb + r0 * BROW + ((ch ^ (((r0 & 3) << 2) | ((r0 >> 2) & 3))) << 4) + 8 * (pp & 1));
        bf16x4 hi = tr_read(sb + r1 * BROW + ((ch ^ (((r1 & 3) << 2) | ((r1 >> 2) & 3))) << 4) + 8 * (pp & 1));
        bfr[b] = cat8(lo, hi);
      }
#pragma unroll
      for (int a = 0; a < 4; ++a)
#pragma unroll
        for (int b = 0; b < NT; ++b) acc[a][b] = mfma16(bfr[b], af[a], acc[a][b]);
    }
    asm volatile("s_waitcnt lgkmcnt(0)" ::: "memory");
  }
#pragma unroll
  for (int a = 0; a < 4; ++a) {
    int m = m0 + wm * 64 + a * 16 + fr;
    if (EPI == EPI_GLU) {
#pragma unroll
      for (int b = 0; b < NT / 2; ++b) {
        int n = nb + wn * (BN / 4) + b * 16 + g * 4;
        float o[4];
#pragma unroll
        for (int r = 0; r < 4; ++r) o[r] = rbf(acc[a][b][r]) * rbf(acc[a][b + NT / 2][r]);
        u32x2 w = {pack2(o[0], o[1]), pack2(o[2], o[3])};
        *(u32x2*)(Cp + (size_t)m * p.ldc + n) = w;
      }
    } else {
#pragma unroll
      for (int b = 0; b < NT; ++b) {
        int n = nb + wn * (BN / 2) + b * 16 + g * 4;
        float o[4];
#pragma unroll
        for (int r = 0; r < 4; ++r) o[r] = acc[a][b][r];
        if (EPI == EPI_RESID) {
          u32x2 rv = *(const u32x2*)(p.R + (size_t)m * p.ldc + n);
          o[0] = rbf(o[0]) + bf2f((u16)(rv[0] & 0xffff)); o[1] = rbf(o[1]) + bf2f((u16)(rv[0] >> 16));
          o[2] = rbf(o[2]) + bf2f((u16)(rv[1] & 0xffff)); o[3] = rbf(o[3]) + bf2f((u16)(rv[1] >> 16));
        }
        u32x2 w = {pack2(o[0], o[1]), pack2(o[2], o[3])};
        *(u32x2*)(Cp + (size_t)m * p.ldc + n) = w;
      }
    }
  }
  __builtin_amdgcn_s_barrier();
}

template <int BN, int EPI>
__global__ __launch_bounds__(512, 2) void k_gemm(GemmP p, int nTilesN, int nStep) {
  extern __shared__ __attribute__((aligned(16))) char smem[];
  int t = blockIdx.x;
  int tm = t / nTilesN, tn = t % nTilesN;
  gemm_tile<BN, EPI>(p, tm * 256, tn * nStep, smem);
}

struct ChainP {
  const u16* mem0; const u16* Wq; const u16* Wk; const u16* Wv; const u16* Wo;
  const u16* kx; const u16* vx; u16* km; u16* vm;
  u64* gB; u64* gA1; u64* gA2; u32* ctl;
};
constexpr u32 SPIN_CAP = 1u << 17;
__device__ __forceinline__ bool spin_fail(u32& spins, u32* ctl) {
  ++spins;
  if (spins > SPIN_CAP) { __hip_atomic_store(ctl, 1u, __ATOMIC_RELAXED, __HIP_MEMORY_SCOPE_AGENT); return true; }
  return (spins & 255u) == 0u && __builtin_amdgcn_readfirstlane(__hip_atomic_load(ctl, __ATOMIC_RELAXED, __HIP_MEMORY_SCOPE_AGENT)) != 0u;
}

constexpr int CL_MEM = 0;
constexpr int CL_RED = 10368;
constexpr int CL_Q   = CL_RED + 36864;
constexpr int CL_K   = CL_Q + 1152;
constexpr int CL_V   = CL_K + 2 * 6912;
constexpr int CL_S   = CL_V + 2 * 25600;
constexpr int CL_O   = CL_S + 2048;
constexpr int CL_END = CL_O + 3200;
constexpr int CL_STG = CL_RED;
constexpr int CHAIN_LDS = (CL_END > CL_STG + 61440 ? CL_END : CL_STG + 61440);

__device__ __forceinline__ u32x4 ld_sc1_16(__amdgpu_buffer_rsrc_t r, u32 off) {
  return __builtin_amdgcn_raw_buffer_load_b128(r, off, 0, 16);
}
__device__ __forceinline__ void st_sc1_16(__amdgpu_buffer_rsrc_t r, u32 off, u32x4 v) {
  __builtin_amdgcn_raw_buffer_store_b128(v, r, off, 0, 16);
}

__global__ __launch_bounds__(512, 2) void k_chain(ChainP p) {
  extern __shared__ __attribute__((aligned(16))) char smem[];
  const int cu = blockIdx.x, h = cu >> 2, j = cu & 3;
  const int tid = threadIdx.x, lane = tid & 63;
  const int wave = __builtin_amdgcn_readfirstlane(tid >> 6);
  const int fr = lane & 15, g = lane >> 4, q4 = fr >> 2, p4 = fr & 3;
  const int kq = wave & 3, grp = wave >> 2;

  __amdgpu_buffer_rsrc_t rB = __builtin_amdgcn_make_buffer_rsrc(p.gB, 0, 128 * 512 * 8, 0x00020000);
  __amdgpu_buffer_rsrc_t rA1 = __builtin_amdgcn_make_buffer_rsrc(p.gA1, 0, 128 * 32 * 40 * 8, 0x00020000);
  __amdgpu_buffer_rsrc_t rA2 = __builtin_amdgcn_make_buffer_rsrc(p.gA2, 0, 2560 * 8, 0x00020000);

  bf16x8 Wf[5][5];
  bf16x8 Wof[2][6];
  {
    u16* stg = (u16*)(smem + CL_STG);
#pragma unroll 1
    for (int mat = 0; mat < 3; ++mat) {
      const u16* W = mat == 0 ? p.Wq : (mat == 1 ? p.Wk : p.Wv);
      __syncthreads();
      for (int c = tid; c < 640 * 6; c += 512) {
        int d = c / 6, ch = c % 6;
        *(u32x4*)(stg + d * 48 + ch * 8) = *(const u32x4*)(W + (size_t)d * HE_ + h * E_ + j * 48 + ch * 8);
      }
      __syncthreads();
#pragma unroll
      for (int i = 0; i < 5; ++i) {
        int T = grp * 5 + i;
        if (T < 9 && T / 3 == mat) {
          int e0 = (T % 3) * 16;
#pragma unroll
          for (int ks = 0; ks < 5; ++ks) {
            int r0 = (kq * 5 + ks) * 32 + g * 8 + q4;
            bf16x4 lo = tr_read(stg + r0 * 48 + e0 + p4 * 4);
            bf16x4 hi = tr_read(stg + (r0 + 4) * 48 + e0 + p4 * 4);
            Wf[i][ks] = cat8(lo, hi);
          }
        }
      }
    }
    __syncthreads();
    for (int c = tid; c < 192 * 20; c += 512) {
      int e = c / 20, ch = c % 20;
      *(u32x4*)(stg + e * 160 + ch * 8) = *(const u32x4*)(p.Wo + (size_t)(h * E_ + e) * D_ + j * 160 + ch * 8);
    }
    __syncthreads();
#pragma unroll
    for (int i = 0; i < 2; ++i) {
      int ct = wave + 8 * i;
      int cc = ct < 10 ? ct : 0;
#pragma unroll
      for (int t = 0; t < 6; ++t) {
        int r0 = t * 32 + g * 8 + q4;
        bf16x4 lo = tr_read(stg + r0 * 160 + cc * 16 + p4 * 4);
        bf16x4 hi = tr_read(stg + (r0 + 4) * 160 + cc * 16 + p4 * 4);
        Wof[i][t] = cat8(lo, hi);
      }
    }
    __syncthreads();
  }
  for (int i = tid; i < (CL_S - CL_Q) / 4; i += 512) ((u32*)(smem + CL_Q))[i] = 0u;
  __syncthreads();

  u16* memL = (u16*)(smem + CL_MEM);
  f32x4* red = (f32x4*)(smem + CL_RED);
  u16* qL = (u16*)(smem + CL_Q);
  float* sL = (float*)(smem + CL_S);
  u16* oL = (u16*)(smem + CL_O);

  auto xload = [&](int s, int c) -> u32x4 {
    if (c < 192) { int tok = c / 6, ch = c % 6; return *(const u32x4*)(p.kx + (size_t)(s * SEG_ + tok) * HE_ + h * E_ + j * 48 + ch * 8); }
    else { int c2 = c - 192; int tok = c2 / 24, ch = c2 % 24; return *(const u32x4*)(p.vx + (size_t)(s * SEG_ + tok) * HE_ + h * E_ + ch * 8); }
  };
  auto xstore = [&](int buf, int c, u32x4 v) {
    if (c < 192) { int tok = c / 6, ch = c % 6; *(u32x4*)(smem + CL_K + buf * 6912 + ((8 + tok) * 72 + ch * 8) * 2) = v; }
    else { int c2 = c - 192; int tok = c2 / 24, ch = c2 % 24; *(u32x4*)(smem + CL_V + buf * 25600 + ((8 + tok) * 200 + ch * 8) * 2) = v; }
  };
  {
    u32x4 v0 = xload(0, tid); xstore(0, tid, v0);
    if (tid + 512 < 960) { u32x4 v1 = xload(0, tid + 512); xstore(0, tid + 512, v1); }
  }
  bool dead = false;

#pragma unroll 1
  for (int s = 0; s < NSEG_; ++s) {
    const u32 ep = (u32)s + 1u;
    const int buf = s & 1;
    u16* kL = (u16*)(smem + CL_K + buf * 6912);
    u16* vL = (u16*)(smem + CL_V + buf * 25600);
    if (s == 0) {
      for (int c = tid; c < 8 * 80; c += 512) { int r = c / 80, ch = c % 80; *(u32x4*)(memL + r * 648 + ch * 8) = *(const u32x4*)(p.mem0 + r * D_ + ch * 8); }
    } else {
      const u32 epm = (u32)s;
      u32x4 a0, a1, a2; u32 spins = 0;
      const bool has2 = tid < 256;
      for (;;) {
        asm volatile("" ::: "memory");
        a0 = ld_sc1_16(rA2, tid * 16); a1 = ld_sc1_16(rA2, (tid + 512) * 16);
        a2 = ld_sc1_16(rA2, has2 ? (tid + 1024) * 16 : tid * 16);
        bool ok = a0[1] == epm && a0[3] == epm && a1[1] == epm && a1[3] == epm && a2[1] == epm && a2[3] == epm;
        if (__all(ok)) break;
        if (dead || spin_fail(spins, p.ctl)) { dead = true; break; }
      }
      { int f = tid * 4; int r = f / 640, c = f % 640; *(u32x2*)(memL + r * 648 + c) = (u32x2){a0[0], a0[2]}; }
      { int f = (tid + 512) * 4; int r = f / 640, c = f % 640; *(u32x2*)(memL + r * 648 + c) = (u32x2){a1[0], a1[2]}; }
      if (has2) { int f = (tid + 1024) * 4; int r = f / 640, c = f % 640; *(u32x2*)(memL + r * 648 + c) = (u32x2){a2[0], a2[2]}; }
    }
    u32x4 xp0, xp1;
    const bool pf = s + 1 < NSEG_;
    if (pf) { xp0 = xload(s + 1, tid); if (tid + 512 < 960) xp1 = xload(s + 1, tid + 512); }
    __syncthreads();
    {
      f32x4 acc[5];
#pragma unroll
      for (int i = 0; i < 5; ++i) acc[i] = (f32x4){0.f, 0.f, 0.f, 0.f};
#pragma unroll
      for (int ks = 0; ks < 5; ++ks) {
        bf16x8 b = *(const bf16x8*)(memL + (fr & 7) * 648 + (kq * 5 + ks) * 32 + g * 8);
#pragma unroll
        for (int i = 0; i < 5; ++i) if (i < 4 || grp == 0) acc[i] = mfma16(Wf[i][ks], b, acc[i]);
      }
#pragma unroll
      for (int i = 0; i < 5; ++i) if (i < 4 || grp == 0) red[(kq * 9 + grp * 5 + i) * 64 + lane] = acc[i];
    }
    __syncthreads();
    if (tid < 288) {
      int T = tid >> 5, gg = (tid >> 3) & 3, r = tid & 7; int l = gg * 16 + r;
      f32x4 a = red[(0 * 9 + T) * 64 + l], b = red[(1 * 9 + T) * 64 + l], c = red[(2 * 9 + T) * 64 + l], d = red[(3 * 9 + T) * 64 + l];
      f32x4 v = (a + b) + (c + d);
      u32x2 w = {pack2(v[0], v[1]), pack2(v[2], v[3])};
      int mat = T / 3, e = (T % 3) * 16 + gg * 4;
      if (mat == 0) *(u32x2*)(qL + r * 72 + e) = w;
      else if (mat == 1) {
        *(u32x2*)(kL + r * 72 + e) = w;
        *(u32x2*)(p.km + ((size_t)(s * M_ + r)) * HE_ + h * E_ + j * 48 + e) = w;
      } else {
        *(u32x2*)(p.vm + ((size_t)(s * M_ + r)) * HE_ + h * E_ + j * 48 + e) = w;
        st_sc1_16(rB, (u32)(cu * 512 + 320 + (r * 48 + e) / 2) * 8, (u32x4){w[0], ep, w[1], ep});
      }
    }
    if (s == NSEG_ - 1) break;
    __syncthreads();
    if (wave < 3) {
      f32x4 acc = {0.f, 0.f, 0.f, 0.f};
#pragma unroll
      for (int kk = 0; kk < 2; ++kk) {
        bf16x8 a = *(const bf16x8*)(kL + (wave * 16 + fr) * 72 + kk * 32 + g * 8);
        bf16x8 b = *(const bf16x8*)(qL + (fr & 7) * 72 + kk * 32 + g * 8);
        acc = mfma16(a, b, acc);
      }
      int key = wave * 16 + g * 4;
      if (fr < 8 && key < 40) {
        u32 o = (u32)(cu * 512 + fr * 40 + key) * 8;
        st_sc1_16(rB, o, (u32x4){__float_as_uint(acc[0]), ep, __float_as_uint(acc[1]), ep});
        st_sc1_16(rB, o + 16, (u32x4){__float_as_uint(acc[2]), ep, __float_as_uint(acc[3]), ep});
      }
    }
    if (tid < 256) {
      u32x4 a[4]; u32 spins = 0;
      for (;;) {
        asm volatile("" ::: "memory");
        bool ok = true;
#pragma unroll
        for (int i = 0; i < 4; ++i) { a[i] = ld_sc1_16(rB, (u32)((h * 4 + i) * 512 + tid * 2) * 8); ok = ok && a[i][1] == ep && a[i][3] == ep; }
        if (__all(ok)) break;
        if (dead || spin_fail(spins, p.ctl)) { dead = true; break; }
      }
      if (tid < 160) {
        float s0 = (__uint_as_float(a[0][0]) + __uint_as_float(a[1][0])) + (__uint_as_float(a[2][0]) + __uint_as_float(a[3][0]));
        float s1 = (__uint_as_float(a[0][2]) + __uint_as_float(a[1][2])) + (__uint_as_float(a[2][2]) + __uint_as_float(a[3][2]));
        const float scale = 0.07216878364870322f;
        int f = tid * 2; int r = f / 40, key = f % 40;
        sL[r * 64 + key] = rbf(rbf(s0) * scale); sL[r * 64 + key + 1] = rbf(rbf(s1) * scale);
      } else {
        int f = (tid - 160) * 4; int r = f / 48, e = f % 48;
#pragma unroll
        for (int i = 0; i < 4; ++i) *(u32x2*)(vL + r * 200 + i * 48 + e) = (u32x2){a[i][0], a[i][2]};
      }
    }
    __syncthreads();
    {
      const int r = fr & 7;
      float sc[16];
      f32x4 s0 = *(const f32x4*)(sL + r * 64 + g * 8), s1 = *(const f32x4*)(sL + r * 64 + g * 8 + 4);
#pragma unroll
      for (int i = 0; i < 4; ++i) { sc[i] = s0[i]; sc[4 + i] = s1[i]; }
      f32x4 s2 = *(const f32x4*)(sL + r * 64 + 32), s3 = *(const f32x4*)(sL + r * 64 + 36);
      float mx = -3.0e38f;
#pragma unroll
      for (int i = 0; i < 4; ++i) { sc[8 + i] = s2[i]; sc[12 + i] = s3[i]; }
#pragma unroll
      for (int i = 0; i < 16; ++i) mx = fmaxf(mx, sc[i]);
      mx = fmaxf(mx, __shfl_xor(mx, 16)); mx = fmaxf(mx, __shfl_xor(mx, 32));
      float sum = 0.f;
#pragma unroll
      for (int i = 0; i < 8; ++i) { sc[i] = __expf(sc[i] - mx); sum += sc[i]; }
      float sum2 = 0.f;
#pragma unroll
      for (int i = 8; i < 16; ++i) { sc[i] = __expf(sc[i] - mx); sum2 += sc[i]; }
      sum += __shfl_xor(sum, 16); sum += __shfl_xor(sum, 32);
      sum += sum2;
      float inv = 1.0f / sum;
      bf16x8 pb0, pb1;
#pragma unroll
      for (int i = 0; i < 8; ++i) { pb0[i] = (short)f2bf(sc[i] * inv); pb1[i] = g == 0 ? (short)f2bf(sc[8 + i] * inv) : (short)0; }
#pragma unroll
      for (int i = 0; i < 2; ++i) {
        int et = wave + 8 * i;
        if (et < 12) {
          f32x4 acc = {0.f, 0.f, 0.f, 0.f};
          {
            int r0 = g * 8 + q4;
            bf16x8 a = cat8(tr_read(vL + r0 * 200 + et * 16 + p4 * 4), tr_read(vL + (r0 + 4) * 200 + et * 16 + p4 * 4));
            acc = mfma16(a, pb0, acc);
            r0 += 32;
            a = cat8(tr_read(vL + r0 * 200 + et * 16 + p4 * 4), tr_read(vL + (r0 + 4) * 200 + et * 16 + p4 * 4));
            acc = mfma16(a, pb1, acc);
          }
          if (fr < 8) *(u32x2*)(oL + fr * 200 + et * 16 + g * 4) = (u32x2){pack2(acc[0], acc[1]), pack2(acc[2], acc[3])};
        }
      }
    }
    __syncthreads();
    {
      bf16x8 ob[6];
#pragma unroll
      for (int t = 0; t < 6; ++t) ob[t] = *(const bf16x8*)(oL + (fr & 7) * 200 + t * 32 + g * 8);
#pragma unroll
      for (int i = 0; i < 2; ++i) {
        int ct = wave + 8 * i;
        if (ct < 10) {
          f32x4 acc = {0.f, 0.f, 0.f, 0.f};
#pragma unroll
          for (int t = 0; t < 6; ++t) acc = mfma16(Wof[i][t], ob[t], acc);
          if (fr < 8) {
            int c = j * 160 + ct * 16 + g * 4;
            int rho = fr * 16 + c / 40, ci = c % 40;
            u32 o = (u32)((rho * 32 + h) * 40 + ci) * 8;
            st_sc1_16(rA1, o, (u32x4){__float_as_uint(acc[0]), ep, __float_as_uint(acc[1]), ep});
            st_sc1_16(rA1, o + 16, (u32x4){__float_as_uint(acc[2]), ep, __float_as_uint(acc[3]), ep});
          }
        }
      }
    }
    if (pf) { xstore(buf ^ 1, tid, xp0); if (tid + 512 < 960) xstore(buf ^ 1, tid + 512, xp1); }
    if (wave == 0) {
      const int pp = lane % 20, gg = lane / 20;
      const u32 rbase = (u32)((cu * 32 + gg) * 40 + pp * 2) * 8;
      const bool v10 = gg < 2, vl = gg < 3;
      u32x4 a[11]; u32 spins = 0;
      for (;;) {
        asm volatile("" ::: "memory");
        bool ok = true;
#pragma unroll
        for (int i = 0; i < 11; ++i) {
          a[i] = __builtin_amdgcn_raw_buffer_load_b128(rA1, rbase, i * 960, 16);
          bool t = a[i][1] == ep && a[i][3] == ep;
          ok = ok && (t || (i == 10 && !v10));
        }
        if (__all(ok || !vl)) break;
        if (dead || spin_fail(spins, p.ctl)) { dead = true; break; }
      }
      float s0 = 0.f, s1 = 0.f;
#pragma unroll
      for (int i = 0; i < 10; ++i) { s0 += __uint_as_float(a[i][0]); s1 += __uint_as_float(a[i][2]); }
      if (v10) { s0 += __uint_as_float(a[10][0]); s1 += __uint_as_float(a[10][2]); }
      float t0 = __shfl(s0, pp + 20), t1 = __shfl(s1, pp + 20), u0 = __shfl(s0, pp + 40), u1 = __shfl(s1, pp + 40);
      s0 = (s0 + t0) + u0; s1 = (s1 + t1) + u1;
      u32 w = pack2(s0, s1);
      u32 wn = __shfl(w, lane + 1);
      if (lane < 20 && (lane & 1) == 0) {
        int r = cu >> 4, c0 = (cu & 15) * 40;
        u32 gi = (u32)(r * 640 + c0) / 2 + lane;
        st_sc1_16(rA2, gi * 8, (u32x4){w, ep, wn, ep});
      }
    }
  }
}

__global__ __launch_bounds__(256) void k_attn(const u16* __restrict__ qx, const u16* __restrict__ kx, const u16* __restrict__ vx,
                                              const u16* __restrict__ km, const u16* __restrict__ vm, u16* __restrict__ ox) {
  __shared__ __attribute__((aligned(16))) u16 vS[4][48 * 200];
  const int wave = threadIdx.x >> 6, lane = threadIdx.x & 63;
  const int task = blockIdx.x * 4 + wave; const int s = task >> 5, h = task & 31;
  const int fr = lane & 15, g = lane >> 4, q4 = fr >> 2, p4 = fr & 3;
  u16* vL = vS[wave];
  for (int c = lane; c < 48 * 24; c += 64) {
    int row = c / 24, ch = c % 24; u32x4 v = {0u, 0u, 0u, 0u};
    if (row < 8) v = *(const u32x4*)(vm + ((size_t)(s * M_ + row)) * HE_ + h * E_ + ch * 8);
    else if (row < 40) v = *(const u32x4*)(vx + ((size_t)(s * SEG_ + row - 8)) * HE_ + h * E_ + ch * 8);
    *(u32x4*)(vL + row * 200 + ch * 8) = v;
  }
  f32x4 sc[3][2];
#pragma unroll
  for (int a = 0; a < 3; ++a) { sc[a][0] = (f32x4){0.f, 0.f, 0.f, 0.f}; sc[a][1] = (f32x4){0.f, 0.f, 0.f, 0.f}; }
  const u16* kp[3];
#pragma unroll
  for (int a = 0; a < 3; ++a) {
    int key = a * 16 + fr; if (key > 39) key = 39;
    kp[a] = key < 8 ? km + ((size_t)(s * M_ + key)) * HE_ + h * E_ : kx + ((size_t)(s * SEG_ + key - 8)) * HE_ + h * E_;
  }
  const u16* qp0 = qx + ((size_t)(s * SEG_ + fr)) * HE_ + h * E_;
  const u16* qp1 = qp0 + (size_t)16 * HE_;
#pragma unroll
  for (int kk = 0; kk < 6; ++kk) {
    bf16x8 b0 = *(const bf16x8*)(qp0 + kk * 32 + g * 8), b1 = *(const bf16x8*)(qp1 + kk * 32 + g * 8);
#pragma unroll
    for (int a = 0; a < 3; ++a) {
      bf16x8 av = *(const bf16x8*)(kp[a] + kk * 32 + g * 8);
      sc[a][0] = mfma16(av, b0, sc[a][0]); sc[a][1] = mfma16(av, b1, sc[a][1]);
    }
  }
  const float scale = 0.07216878364870322f;
  bf16x8 pb[2][2];
#pragma unroll
  for (int qt = 0; qt < 2; ++qt) {
    int qrow = qt * 16 + fr;
    float v[12]; float mx = -3.0e38f;
#pragma unroll
    for (int a = 0; a < 3; ++a)
#pragma unroll
      for (int r = 0; r < 4; ++r) {
        int key = a * 16 + g * 4 + r;
        float x = rbf(rbf(sc[a][qt][r]) * scale);
        bool vis = key < 8 || (key - 8) <= qrow;
        x = vis ? x : rbf(-1000000000.0f);
        v[a * 4 + r] = x;
        if (key < 40) mx = fmaxf(mx, x);
      }
    mx = fmaxf(mx, __shfl_xor(mx, 16)); mx = fmaxf(mx, __shfl_xor(mx, 32));
    float sum = 0.f;
#pragma unroll
    for (int a = 0; a < 3; ++a)
#pragma unroll
      for (int r = 0; r < 4; ++r) {
        int key = a * 16 + g * 4 + r;
        float e = key < 40 ? __expf(v[a * 4 + r] - mx) : 0.f;
        v[a * 4 + r] = e; sum += e;
      }
    sum += __shfl_xor(sum, 16); sum += __shfl_xor(sum, 32);
    float inv = 1.0f / sum;
#pragma unroll
    for (int r = 0; r < 4; ++r) {
      pb[qt][0][r] = (short)f2bf(v[r] * inv); pb[qt][0][4 + r] = (short)f2bf(v[4 + r] * inv);
      pb[qt][1][r] = (short)f2bf(v[8 + r] * inv); pb[qt][1][4 + r] = 0;
    }
  }
  __syncthreads();
#pragma unroll
  for (int et = 0; et < 12; ++et) {
    int r0 = g * 4 + q4;
    bf16x8 a0 = cat8(tr_read(vL + r0 * 200 + et * 16 + p4 * 4), tr_read(vL + (16 + r0) * 200 + et * 16 + p4 * 4));
    bf16x4 t2 = tr_read(vL + (32 + r0) * 200 + et * 16 + p4 * 4);
    bf16x8 a1 = cat8(t2, t2);
#pragma unroll
    for (int qt = 0; qt < 2; ++qt) {
      f32x4 acc = {0.f, 0.f, 0.f, 0.f};
      acc = mfma16(a0, pb[qt][0], acc);
      acc = mfma16(a1, pb[qt][1], acc);
      int tok = s * SEG_ + qt * 16 + fr;
      *(u32x2*)(ox + (size_t)tok * HE_ + h * E_ + et * 16 + g * 4) = (u32x2){pack2(acc[0], acc[1]), pack2(acc[2], acc[3])};
    }
  }
}

extern "C" void kernel_launch(void* const* d_in, const int* in_sizes, int n_in,
                              void* d_out, int out_size, void* d_ws, size_t ws_size,
                              hipStream_t stream) {
  const u16* x = (const u16*)d_in[0];
  const u16* mem0 = (const u16*)d_in[1];
  const u16* Wq = (const u16*)d_in[2];
  const u16* Wk = (const u16*)d_in[3];
  const u16* Wv = (const u16*)d_in[4];
  const u16* Wo = (const u16*)d_in[5];
  const u16* wg = (const u16*)d_in[8];
  const u16* wu = (const u16*)d_in[9];
  const u16* wd = (const u16*)d_in[10];
  u16* out = (u16*)d_out;
  char* ws = (char*)d_ws;
  u16* xn = (u16*)(ws + WS_XN); u16* qx = (u16*)(ws + WS_QX); u16* kx = (u16*)(ws + WS_KX); u16* vx = (u16*)(ws + WS_VX);
  u16* km = (u16*)(ws + WS_KM); u16* vm = (u16*)(ws + WS_VM); u16* ox = (u16*)(ws + WS_OX);
  u16* hb = (u16*)(ws + WS_H); u16* hn = (u16*)(ws + WS_HN); u16* gu = (u16*)(ws + WS_GU);

  static bool attr_done = false;
  if (!attr_done) {
    hipFuncSetAttribute((const void*)k_gemm<256, EPI_STORE>, hipFuncAttributeMaxDynamicSharedMemorySize, 131072);
    hipFuncSetAttribute((const void*)k_gemm<128, EPI_RESID>, hipFuncAttributeMaxDynamicSharedMemorySize, 98304);
    hipFuncSetAttribute((const void*)k_gemm<256, EPI_GLU>, hipFuncAttributeMaxDynamicSharedMemorySize, 131072);
    hipFuncSetAttribute((const void*)k_chain, hipFuncAttributeMaxDynamicSharedMemorySize, CHAIN_LDS);
    attr_done = true;
  }

  hipMemsetAsync(ws, 0, WS_ZEND, stream);
  k_ln<<<S_ / 4, 256, 0, stream>>>(x, xn, S_);
  {
    GemmP p{xn, Wq, Wk, Wv, qx, kx, vx, nullptr, D_, HE_, HE_, D_, HE_, 0};
    k_gemm<256, EPI_STORE><<<8 * 72, 512, 131072, stream>>>(p, 72, 256);
  }
  {
    ChainP p{mem0, Wq, Wk, Wv, Wo, kx, vx, km, vm, (u64*)(ws + WS_GB), (u64*)(ws + WS_GA1), (u64*)(ws + WS_GA2), (u32*)(ws + WS_CTL)};
    k_chain<<<128, 512, CHAIN_LDS, stream>>>(p);
  }
  k_attn<<<NSEG_ * H_ / 4, 256, 0, stream>>>(qx, kx, vx, km, vm, ox);
  {
    GemmP p{ox, Wo, Wo, Wo, hb, hb, hb, x, HE_, D_, D_, HE_, D_, 0};
    k_gemm<128, EPI_RESID><<<8 * 5, 512, 98304, stream>>>(p, 5, 128);
  }
  k_ln<<<S_ / 4, 256, 0, stream>>>(hb, hn, S_);
  {
    GemmP p{hn, wg, wu, wu, gu, gu, gu, nullptr, D_, F_, F_, D_, F_, 0};
    k_gemm<256, EPI_GLU><<<8 * 12, 512, 131072, stream>>>(p, 12, 128);
  }
  {
    GemmP p{gu, wd, wd, wd, out, out, out, hb, F_, D_, D_, F_, D_, 0};
    k_gemm<128, EPI_RESID><<<8 * 5, 512, 98304, stream>>>(p, 5, 128);
  }
}
```

```cpp
#include <hip/hip_runtime.h>
#include <hip/hip_bf16.h>
#include <stdint.h>

typedef unsigned short u16;
typedef unsigned int u32;
typedef unsigned long long u64;
typedef short bf16x8 __attribute__((ext_vector_type(8)));
typedef short bf16x4 __attribute__((ext_vector_type(4)));
typedef float f32x4 __attribute__((ext_vector_type(4)));
typedef u32 u32x4 __attribute__((ext_vector_type(4)));
typedef u32 u32x2 __attribute__((ext_vector_type(2)));
#define LAS __attribute__((address_space(3)))
#define RLX_AGENT __ATOMIC_RELAXED, __HIP_MEMORY_SCOPE_AGENT

constexpr int S_ = 2048, D_ = 640, H_ = 32, E_ = 192, HE_ = H_ * E_;
constexpr int M_ = 8, SEG_ = 32, NSEG_ = 64, F_ = 1536;

constexpr u32 WS_CTL   = 0;
constexpr u32 WS_FQKV  = 4096;
constexpr u32 WS_FCH   = WS_FQKV + 8 * 96 * 64;
constexpr u32 WS_FAT   = WS_FCH + 128 * 64;
constexpr u32 WS_FOP   = WS_FAT + 64 * 64;
constexpr u32 WS_FLN   = WS_FOP + 8 * 64;
constexpr u32 WS_FGL   = WS_FLN + 8 * 64;
constexpr u32 WS_GA2   = (WS_FGL + 8 * 64 + 4095) / 4096 * 4096;
constexpr int A2_REP = 16, A2_STRIDE = 20736;
constexpr u32 WS_GB    = WS_GA2 + A2_REP * A2_STRIDE;
constexpr u32 WS_GA1   = WS_GB + 128 * 3072;
constexpr u32 WS_ZEND  = WS_GA1 + 40 * 32 * 512;
constexpr u32 WS_XN    = (WS_ZEND + 4095) / 4096 * 4096;
constexpr u32 WS_KX    = WS_XN + S_ * D_ * 2;
constexpr u32 WS_VX    = WS_KX + S_ * HE_ * 2;
constexpr u32 WS_QX    = WS_VX + S_ * HE_ * 2;
constexpr u32 WS_KM    = WS_QX + S_ * HE_ * 2;
constexpr u32 WS_VM    = WS_KM + NSEG_ * M_ * HE_ * 2;
constexpr u32 WS_OX    = WS_VM + NSEG_ * M_ * HE_ * 2;
constexpr u32 WS_H     = WS_OX + S_ * HE_ * 2;
constexpr u32 WS_HN    = WS_H + S_ * D_ * 2;
constexpr u32 WS_GU    = WS_HN + S_ * D_ * 2;
constexpr u32 WS_SLAB  = WS_GU + S_ * F_ * 2;
constexpr u32 WS_END   = WS_SLAB + 8u * S_ * D_ * 4;

__device__ __forceinline__ float bf2f(u16 v) { return __uint_as_float(((u32)v) << 16); }
__device__ __forceinline__ u16 f2bf(float f) {
  __hip_bfloat16 b = __float2bfloat16(f);
  return *reinterpret_cast<u16*>(&b);
}
__device__ __forceinline__ float rbf(float f) { return bf2f(f2bf(f)); }
__device__ __forceinline__ u32 pack2(float a, float b) { return (u32)f2bf(a) | ((u32)f2bf(b) << 16); }
__device__ __forceinline__ f32x4 mfma16(bf16x8 a, bf16x8 b, f32x4 c) {
  return __builtin_amdgcn_mfma_f32_16x16x32_bf16(a, b, c, 0, 0, 0);
}
__device__ __forceinline__ bf16x4 tr_read(const void* p) {
  return __builtin_amdgcn_ds_read_tr16_b64_v4i16((LAS bf16x4*)p);
}
__device__ __forceinline__ bf16x8 cat8(bf16x4 a, bf16x4 b) {
  bf16x8 r; r[0]=a[0]; r[1]=a[1]; r[2]=a[2]; r[3]=a[3]; r[4]=b[0]; r[5]=b[1]; r[6]=b[2]; r[7]=b[3]; return r;
}
__device__ __forceinline__ void lbar() { asm volatile("s_waitcnt lgkmcnt(0)\n\ts_barrier" ::: "memory"); }
typedef __amdgpu_buffer_rsrc_t rsrc_t;
__device__ __forceinline__ u32x4 ld_sc1_16(rsrc_t r, u32 off) { return __builtin_amdgcn_raw_buffer_load_b128(r, off, 0, 16); }
__device__ __forceinline__ void st_sc1_16(rsrc_t r, u32 off, u32x4 v) { __builtin_amdgcn_raw_buffer_store_b128(v, r, off, 0, 16); }
__device__ __forceinline__ void st_sc1_8(rsrc_t r, u32 off, u32x2 v) { __builtin_amdgcn_raw_buffer_store_b64(v, r, off, 0, 16); }
__device__ __forceinline__ float red16_max(float x) {
  auto r = __builtin_amdgcn_permlane16_swap(__float_as_uint(x), __float_as_uint(x), false, false);
  return fmaxf(__uint_as_float(r[0]), __uint_as_float(r[1]));
}
__device__ __forceinline__ float red32_max(float x) {
  auto r = __builtin_amdgcn_permlane32_swap(__float_as_uint(x), __float_as_uint(x), false, false);
  return fmaxf(__uint_as_float(r[0]), __uint_as_float(r[1]));
}
__device__ __forceinline__ float red16_add(float x) {
  auto r = __builtin_amdgcn_permlane16_swap(__float_as_uint(x), __float_as_uint(x), false, false);
  return __uint_as_float(r[0]) + __uint_as_float(r[1]);
}
__device__ __forceinline__ float red32_add(float x) {
  auto r = __builtin_amdgcn_permlane32_swap(__float_as_uint(x), __float_as_uint(x), false, false);
  return __uint_as_float(r[0]) + __uint_as_float(r[1]);
}

constexpr u32 SPIN_CAP = 1u << 18;
__device__ __forceinline__ bool spin_fail(u32& spins, u32* ctl) {
  ++spins;
  if (spins > SPIN_CAP) { __hip_atomic_store(ctl, 1u, RLX_AGENT); return true; }
  return (spins & 255u) == 0u && __builtin_amdgcn_readfirstlane(__hip_atomic_load(ctl, RLX_AGENT)) != 0u;
}

struct FP {
  const u16* x; const u16* mem0; const u16* Wq; const u16* Wk; const u16* Wv; const u16* Wo;
  const u16* wg; const u16* wu; const u16* wd; u16* out; char* ws;
};
typedef const __attribute__((address_space(4))) FP* kargp_t;
__device__ __forceinline__ FP getp() {
#if defined(__HIP_DEVICE_COMPILE__)
  kargp_t kp = (kargp_t)__builtin_amdgcn_kernarg_segment_ptr(); asm volatile("" : "+s"(kp)); return *kp;
#else
  return FP{};
#endif
}
constexpr int NTH = 768;
constexpr int LDS_BYTES = 153600;

__device__ __forceinline__ void ln_row(const float (&v)[10], u32 (&o)[5]) {
  float sum = 0.f;
#pragma unroll
  for (int i = 0; i < 10; ++i) sum += v[i];
#pragma unroll
  for (int m = 32; m >= 1; m >>= 1) sum += __shfl_xor(sum, m);
  float mean = sum * (1.0f / D_);
  float sq = 0.f;
#pragma unroll
  for (int i = 0; i < 10; ++i) { float d = v[i] - mean; sq += d * d; }
#pragma unroll
  for (int m = 32; m >= 1; m >>= 1) sq += __shfl_xor(sq, m);
  float var = sq * (1.0f / D_);
  float mb = rbf(mean), vb = rbf(var);
  float den = rbf(sqrtf(rbf(vb + 1e-5f)));
#pragma unroll
  for (int i = 0; i < 5; ++i) o[i] = pack2(rbf(v[2 * i] - mb) / den, rbf(v[2 * i + 1] - mb) / den);
}
__global__ __launch_bounds__(256) void k_ln(const u16* __restrict__ x, u16* __restrict__ y) {
  int row = blockIdx.x * 4 + (threadIdx.x >> 6);
  int lane = threadIdx.x & 63;
  const u32* xr = (const u32*)(x + (size_t)row * D_);
  float v[10];
#pragma unroll
  for (int i = 0; i < 5; ++i) { u32 w = xr[i * 64 + lane]; v[2 * i] = bf2f((u16)(w & 0xffff)); v[2 * i + 1] = bf2f((u16)(w >> 16)); }
  u32 o[5]; ln_row(v, o);
  u32* yr = (u32*)(y + (size_t)row * D_);
#pragma unroll
  for (int i = 0; i < 5; ++i) yr[i * 64 + lane] = o[i];
}

__device__ __forceinline__ void wait_flag(char* ws, u32 foff, u32 target) {
  if (threadIdx.x == 0) {
    u32* f = (u32*)(ws + foff); u32* ctl = (u32*)(ws + WS_CTL); u32 spins = 0;
    while (__hip_atomic_load(f, RLX_AGENT) < target) {
      __builtin_amdgcn_s_sleep(8);
      if (++spins > (1u << 20)) { __hip_atomic_store(ctl, 1u, RLX_AGENT); break; }
      if ((spins & 63u) == 0u && __hip_atomic_load(ctl, RLX_AGENT) != 0u) break;
    }
  }
}
__device__ __forceinline__ void acquire_bar() {
  if (threadIdx.x == 0) { __builtin_amdgcn_fence(__ATOMIC_ACQUIRE, "agent"); asm volatile("s_waitcnt vmcnt(0)" ::: "memory"); }
  __syncthreads();
}
__device__ __forceinline__ void signal_flag(char* ws, u32 foff) {
  asm volatile("s_waitcnt vmcnt(0)" ::: "memory");
  __syncthreads();
  if (threadIdx.x == 0) __hip_atomic_fetch_add((u32*)(ws + foff), 1u, RLX_AGENT);
}

#define TL(blk, i) do {} while (0)
template <int NWN> __device__ __forceinline__ int bswz(int row) {
  if (NWN == 3) return (((row >> 1) & 1) | (((row >> 3) & 1) << 1)) << 1;
  else if (NWN == 2) return ((row & 3) << 2) | ((row >> 2) & 3);
  else return (((row >> 1) & 1) | (((row >> 3) & 1) << 1)) << 1;
}
enum { E_BF16 = 0, E_SLAB = 1, E_GLU = 2, E_DOWN = 3 };
template <int NWM, int NWN, int EPI>
__device__ __forceinline__ void gtile(const u16* __restrict__ A, int lda, const u16* __restrict__ B0, const u16* __restrict__ B1, int ldb,
                                      int nk, int m0, int nb, rsrc_t rW, u32 coff, int ldc, const u16* resid, u16* outp, char* smem) {
  constexpr int BM = NWM * 64, BN = NWN * 64, BROW = BN * 2, NA = BM / 16, NI = NA + BN / 16, ST_BYTES = 28672, NST = 5;
  constexpr int NPW = (NI + 11) / 12;
  int tz = threadIdx.x; asm volatile("" : "+v"(tz));
  const int tid = tz, lane = tid & 63, wave = __builtin_amdgcn_readfirstlane(tid >> 6);
  const int wm = wave % NWM, wn = wave / NWM;
  const int fr = lane & 15, g = lane >> 4;
  const u16* src[3]; u32 sstep[3];
#pragma unroll
  for (int i = 0; i < 3; ++i) {
    int ii = wave + 12 * i;
    if (ii < NA) { int b = ii * 1024 + lane * 16; int row = b >> 6, pos = (b >> 4) & 3, ch = pos ^ ((4 - (row >> 2)) & 3);
      src[i] = A + (size_t)(m0 + row) * lda + ch * 8; sstep[i] = 32; }
    else { int b = ((ii < NI ? ii : NA) - NA) * 1024 + lane * 16; int row = b / BROW, pos = (b % BROW) >> 4;
      int ch = pos ^ bswz<NWN>(row);
      if (EPI == E_GLU) { int w2 = ch >> 3, within = ch & 7; const u16* base = (within >> 2) ? B1 : B0;
        src[i] = base + (size_t)row * ldb + nb + w2 * 32 + (within & 3) * 8; }
      else src[i] = B0 + (size_t)row * ldb + nb + ch * 8;
      sstep[i] = 32u * (u32)ldb; }
  }
  const int npw = (wave + 12 * (NPW - 1)) < NI ? NPW : NPW - 1;
  auto stage = [&](int kt, int buf) {
    char* sb = smem + buf * ST_BYTES;
#pragma unroll
    for (int i = 0; i < NPW; ++i)
      if (i < npw) __builtin_amdgcn_global_load_lds((const u32*)(src[i] + (size_t)kt * sstep[i]), (LAS u32*)(sb + (wave + 12 * i) * 1024), 16, 0, 0);
  };
  f32x4 acc[4][4];
#pragma unroll
  for (int a = 0; a < 4; ++a)
#pragma unroll
    for (int b = 0; b < 4; ++b) acc[a][b] = (f32x4){0.f, 0.f, 0.f, 0.f};
  const bool comp = wave < NWM * NWN;
  const int nk2 = nk * 2;
  stage(0, 0); stage(1, 1); stage(2, 2); stage(3, 3);
  int cb = 0, pb = 4;
  const int q = fr >> 2, pp = fr & 3;
  const int r0 = g * 8 + q, r1 = r0 + 4;
  const int x0 = bswz<NWN>(r0), x1 = bswz<NWN>(r1);
  const int aoff = (wm * 64 + fr) * 64 + ((g ^ ((4 - (fr >> 2)) & 3)) << 4);
#pragma unroll 1
  for (int kt = 0; kt < nk2; ++kt) {
    if (kt + 4 <= nk2) {
      if (npw == 3) asm volatile("s_waitcnt vmcnt(9)" ::: "memory");
      else if (npw == 2) asm volatile("s_waitcnt vmcnt(6)" ::: "memory");
      else if (npw == 1) asm volatile("s_waitcnt vmcnt(3)" ::: "memory");
    }
    else asm volatile("s_waitcnt vmcnt(0)" ::: "memory");
    lbar();
    if (kt + 4 < nk2) stage(kt + 4, pb);
    if (comp) {
      const char* sa = smem + cb * ST_BYTES; const char* sb = sa + NA * 1024;
      bf16x8 af[4], bfr[4];
#pragma unroll
      for (int a = 0; a < 4; ++a) af[a] = *(const bf16x8*)(sa + aoff + a * 1024);
#pragma unroll
      for (int b = 0; b < 4; ++b) {
        int ch = wn * 8 + b * 2 + (pp >> 1);
        bfr[b] = cat8(tr_read(sb + r0 * BROW + ((ch ^ x0) << 4) + 8 * (pp & 1)), tr_read(sb + r1 * BROW + ((ch ^ x1) << 4) + 8 * (pp & 1)));
      }
#pragma unroll
      for (int a = 0; a < 4; ++a)
#pragma unroll
        for (int b = 0; b < 4; ++b) acc[a][b] = mfma16(bfr[b], af[a], acc[a][b]);
    }
    cb = cb == NST - 1 ? 0 : cb + 1; pb = pb == NST - 1 ? 0 : pb + 1;
  }
  if (EPI == E_DOWN) {
    if (comp) {
#pragma unroll
      for (int a = 0; a < 4; ++a) {
        const int m = m0 + wm * 64 + a * 16 + fr;
#pragma unroll
        for (int b = 0; b < 4; ++b) {
          int n = nb + wn * 64 + b * 16 + g * 4;
          u32x2 rv = *(const u32x2*)(resid + (size_t)m * ldc + n);
          float o0 = rbf(acc[a][b][0]) + bf2f((u16)(rv[0] & 0xffff)), o1 = rbf(acc[a][b][1]) + bf2f((u16)(rv[0] >> 16));
          float o2 = rbf(acc[a][b][2]) + bf2f((u16)(rv[1] & 0xffff)), o3 = rbf(acc[a][b][3]) + bf2f((u16)(rv[1] >> 16));
          *(u32x2*)(outp + (size_t)m * ldc + n) = (u32x2){pack2(o0, o1), pack2(o2, o3)};
        }
      }
    }
  } else {
    constexpr int OC = EPI == E_GLU ? NWN * 32 : BN;
    constexpr int ES = EPI == E_SLAB ? 4 : 2;
    constexpr int RB = OC * ES, RS = RB + 16, CPR = RB / 16;
    lbar();
    if (comp) {
#pragma unroll
      for (int a = 0; a < 4; ++a) {
        char* rowp = smem + (wm * 64 + a * 16 + fr) * RS;
        if (EPI == E_GLU) {
#pragma unroll
          for (int b = 0; b < 2; ++b) {
            float o[4];
#pragma unroll
            for (int r = 0; r < 4; ++r) o[r] = rbf(acc[a][b][r]) * rbf(acc[a][b + 2][r]);
            *(u32x2*)(rowp + (wn * 32 + b * 16 + g * 4) * 2) = (u32x2){pack2(o[0], o[1]), pack2(o[2], o[3])};
          }
        } else {
#pragma unroll
          for (int b = 0; b < 4; ++b) {
            if (EPI == E_BF16) *(u32x2*)(rowp + (wn * 64 + b * 16 + g * 4) * 2) = (u32x2){pack2(acc[a][b][0], acc[a][b][1]), pack2(acc[a][b][2], acc[a][b][3])};
            else *(f32x4*)(rowp + (wn * 64 + b * 16 + g * 4) * 4) = acc[a][b];
          }
        }
      }
    }
    lbar();
#pragma unroll 2
    for (int c = tid; c < BM * CPR; c += NTH) {
      int row = c / CPR, ch = c % CPR;
      u32x4 v = *(const u32x4*)(smem + row * RS + ch * 16);
      st_sc1_16(rW, coff + (u32)((m0 + row) * ldc + nb) * ES + ch * 16, v);
    }
  }
}

__device__ __forceinline__ void attn_wave(const char* ws, rsrc_t rW, int s, int h, u16* vL) {
  int tz = threadIdx.x; asm volatile("" : "+v"(tz));
  const int lane = tz & 63, fr = lane & 15, g = lane >> 4, q4 = fr >> 2, p4 = fr & 3;
  const u16* qx = (const u16*)(ws + WS_QX); const u16* kx = (const u16*)(ws + WS_KX); const u16* vx = (const u16*)(ws + WS_VX);
  const u16* km = (const u16*)(ws + WS_KM); const u16* vm = (const u16*)(ws + WS_VM);
  {
    u32x4 vv[15];
#pragma unroll
    for (int i = 0; i < 15; ++i) {
      int c = i * 64 + lane; int row = c / 24, ch = c % 24;
      const u16* sp = row < 8 ? vm + ((size_t)(s * M_ + row)) * HE_ : vx + ((size_t)(s * SEG_ + row - 8)) * HE_;
      vv[i] = *(const u32x4*)(sp + h * E_ + ch * 8);
    }
#pragma unroll
    for (int i = 0; i < 15; ++i) { int c = i * 64 + lane; int row = c / 24, ch = c % 24; *(u32x4*)(vL + row * 200 + ch * 8) = vv[i]; }
#pragma unroll
    for (int i = 15; i < 18; ++i) { int c = i * 64 + lane; int row = c / 24, ch = c % 24; *(u32x4*)(vL + row * 200 + ch * 8) = (u32x4){0u, 0u, 0u, 0u}; }
  }
  f32x4 sc[3][2];
#pragma unroll
  for (int a = 0; a < 3; ++a) { sc[a][0] = (f32x4){0.f, 0.f, 0.f, 0.f}; sc[a][1] = (f32x4){0.f, 0.f, 0.f, 0.f}; }
  const u16* kp[3];
#pragma unroll
  for (int a = 0; a < 3; ++a) {
    int key = a * 16 + fr; if (key > 39) key = 39;
    kp[a] = key < 8 ? km + ((size_t)(s * M_ + key)) * HE_ + h * E_ : kx + ((size_t)(s * SEG_ + key - 8)) * HE_ + h * E_;
  }
  const u16* qp0 = qx + ((size_t)(s * SEG_ + fr)) * HE_ + h * E_;
  const u16* qp1 = qp0 + (size_t)16 * HE_;
#pragma unroll
  for (int kk = 0; kk < 6; ++kk) {
    bf16x8 b0 = *(const bf16x8*)(qp0 + kk * 32 + g * 8), b1 = *(const bf16x8*)(qp1 + kk * 32 + g * 8);
#pragma unroll
    for (int a = 0; a < 3; ++a) {
      bf16x8 av = *(const bf16x8*)(kp[a] + kk * 32 + g * 8);
      sc[a][0] = mfma16(av, b0, sc[a][0]); sc[a][1] = mfma16(av, b1, sc[a][1]);
    }
  }
  const float scale = 0.07216878364870322f;
  bf16x8 pb[2][2];
#pragma unroll
  for (int qt = 0; qt < 2; ++qt) {
    int qrow = qt * 16 + fr;
    float v[12]; float mx = -3.0e38f;
#pragma unroll
    for (int a = 0; a < 3; ++a)
#pragma unroll
      for (int r = 0; r < 4; ++r) {
        int key = a * 16 + g * 4 + r;
        float x = rbf(rbf(sc[a][qt][r]) * scale);
        bool vis = key < 8 || (key - 8) <= qrow;
        x = vis ? x : rbf(-1000000000.0f);
        v[a * 4 + r] = x;
        if (key < 40) mx = fmaxf(mx, x);
      }
    mx = red16_max(mx); mx = red32_max(mx);
    float sum = 0.f;
#pragma unroll
    for (int a = 0; a < 3; ++a)
#pragma unroll
      for (int r = 0; r < 4; ++r) {
        int key = a * 16 + g * 4 + r;
        float e = key < 40 ? __expf(v[a * 4 + r] - mx) : 0.f;
        v[a * 4 + r] = e; sum += e;
      }
    sum = red16_add(sum); sum = red32_add(sum);
    float inv = 1.0f / sum;
#pragma unroll
    for (int r = 0; r < 4; ++r) {
      pb[qt][0][r] = (short)f2bf(v[r] * inv); pb[qt][0][4 + r] = (short)f2bf(v[4 + r] * inv);
      pb[qt][1][r] = (short)f2bf(v[8 + r] * inv); pb[qt][1][4 + r] = 0;
    }
  }
  asm volatile("s_waitcnt lgkmcnt(0)" ::: "memory");
  u32x2 ov[12][2];
#pragma unroll
  for (int et = 0; et < 12; ++et) {
    int r0 = g * 4 + q4;
    bf16x8 a0 = cat8(tr_read(vL + r0 * 200 + et * 16 + p4 * 4), tr_read(vL + (16 + r0) * 200 + et * 16 + p4 * 4));
    bf16x4 t2 = tr_read(vL + (32 + r0) * 200 + et * 16 + p4 * 4);
    bf16x8 a1 = cat8(t2, t2);
#pragma unroll
    for (int qt = 0; qt < 2; ++qt) {
      f32x4 acc = {0.f, 0.f, 0.f, 0.f};
      acc = mfma16(a0, pb[qt][0], acc);
      acc = mfma16(a1, pb[qt][1], acc);
      ov[et][qt] = (u32x2){pack2(acc[0], acc[1]), pack2(acc[2], acc[3])};
    }
  }
  asm volatile("s_waitcnt lgkmcnt(0)" ::: "memory");
#pragma unroll
  for (int et = 0; et < 12; ++et)
#pragma unroll
    for (int qt = 0; qt < 2; ++qt) *(u32x2*)(vL + (qt * 16 + fr) * 200 + et * 16 + g * 4) = ov[et][qt];
  asm volatile("s_waitcnt lgkmcnt(0)" ::: "memory");
#pragma unroll
  for (int i = 0; i < 12; ++i) {
    int c = i * 64 + lane; int row = c / 24, ch = c % 24;
    u32x4 v = *(const u32x4*)(vL + row * 200 + ch * 8);
    st_sc1_16(rW, WS_OX + (u32)((s * SEG_ + row) * HE_ + h * E_ + ch * 8) * 2, v);
  }
}

__device__ __forceinline__ void gemm_role(int gb, char* smem) {
  char* ws = getp().ws;
  rsrc_t rW = __builtin_amdgcn_make_buffer_rsrc(ws, 0, WS_END, 0x00020000);
  const int wave = __builtin_amdgcn_readfirstlane(threadIdx.x >> 6);
  TL(128 + gb, 0);
#pragma unroll 1
  for (int a = gb; a < 768; a += 128) {
    int mb = a / 96, n = a % 96; int mat = n >> 5, h = n & 31;
    const FP p = getp();
    const u16* W = mat == 0 ? p.Wk : (mat == 1 ? p.Wv : p.Wq);
    u32 coff = mat == 0 ? WS_KX : (mat == 1 ? WS_VX : WS_QX);
    gtile<4, 3, E_BF16>((const u16*)(ws + WS_XN), D_, W, W, HE_, 10, mb * 256, h * E_, rW, coff, HE_, nullptr, nullptr, smem);
    signal_flag(ws, WS_FQKV + (u32)(mb * 96 + n) * 64);
    TL(128 + gb, 1 + a / 128);
  }
#pragma unroll 1
  for (int mb = 0; mb < 8; ++mb) {
    const int k = gb;
    if (k < 32) {
      wait_flag(ws, WS_FQKV + (u32)(mb * 96 + 64 + k) * 64, 1);
#pragma unroll 1
      for (int j = 0; j < 4; ++j) wait_flag(ws, WS_FCH + (u32)(k * 4 + j) * 64, (u32)(mb + 1));
      acquire_bar();
      const FP p = getp();
      if (mb == 7) TL(128 + gb, 16);
      if (wave < 8) attn_wave(ws, rW, mb * 8 + wave, k, (u16*)(smem + wave * 19200));
      signal_flag(ws, WS_FAT + (u32)(mb * 8 + (k >> 2)) * 64);
    } else if (k < 72) {
      int t = k - 32, nt = t % 5, ks = t / 5;
      wait_flag(ws, WS_FAT + (u32)(mb * 8 + ks) * 64, 4);
      acquire_bar();
      const FP p = getp();
      if (mb == 7) TL(128 + gb, 16);
      gtile<4, 2, E_SLAB>((const u16*)(ws + WS_OX) + ks * 768, HE_, p.Wo + (size_t)ks * 768 * D_, nullptr, D_, 12, mb * 256, nt * 128, rW,
                       WS_SLAB + (u32)ks * (S_ * D_ * 4), D_, nullptr, nullptr, smem);
      signal_flag(ws, WS_FOP + (u32)mb * 64);
    } else if (k < 83) {
      wait_flag(ws, WS_FOP + (u32)mb * 64, 40);
      acquire_bar();
      const FP p = getp();
      if (mb == 7) TL(128 + gb, 16);
      const int lane = threadIdx.x & 63;
#pragma unroll 1
      for (int rr = (k - 72) * 24 + wave; rr < 256 && rr < (k - 71) * 24; rr += 12) {
        int row = mb * 256 + rr;
        const bool two = lane < 16;
        float v[16];
#pragma unroll
        for (int hf = 0; hf < 2; ++hf) {
          int c0 = (hf == 0 ? lane : (two ? 64 + lane : lane)) * 8;
          const f32x4* sl = (const f32x4*)((const float*)(ws + WS_SLAB) + (size_t)row * D_ + c0);
          f32x4 lo[8], hi[8];
#pragma unroll
          for (int q = 0; q < 8; ++q) { lo[q] = sl[(size_t)q * (S_ * D_ / 4)]; hi[q] = sl[(size_t)q * (S_ * D_ / 4) + 1]; }
          f32x4 s0 = ((lo[0] + lo[1]) + (lo[2] + lo[3])) + ((lo[4] + lo[5]) + (lo[6] + lo[7]));
          f32x4 s1 = ((hi[0] + hi[1]) + (hi[2] + hi[3])) + ((hi[4] + hi[5]) + (hi[6] + hi[7]));
          u32x4 xw = *(const u32x4*)(p.x + (size_t)row * D_ + c0);
          u32x4 hw;
#pragma unroll
          for (int i = 0; i < 2; ++i) {
            hw[i] = pack2(rbf(s0[2 * i]) + bf2f((u16)(xw[i] & 0xffff)), rbf(s0[2 * i + 1]) + bf2f((u16)(xw[i] >> 16)));
            hw[2 + i] = pack2(rbf(s1[2 * i]) + bf2f((u16)(xw[2 + i] & 0xffff)), rbf(s1[2 * i + 1]) + bf2f((u16)(xw[2 + i] >> 16)));
          }
#pragma unroll
          for (int i = 0; i < 4; ++i) { v[hf * 8 + 2 * i] = bf2f((u16)(hw[i] & 0xffff)); v[hf * 8 + 2 * i + 1] = bf2f((u16)(hw[i] >> 16)); }
          if (hf == 0 || two) st_sc1_16(rW, WS_H + (u32)(row * D_ + c0) * 2, hw);
        }
        float sum = 0.f;
#pragma unroll
        for (int i = 0; i < 8; ++i) sum += v[i] + (two ? v[8 + i] : 0.f);
#pragma unroll
        for (int m = 32; m >= 1; m >>= 1) sum += __shfl_xor(sum, m);
        float mean = sum * (1.0f / D_), sq = 0.f;
#pragma unroll
        for (int i = 0; i < 8; ++i) { float d = v[i] - mean, e = v[8 + i] - mean; sq += d * d + (two ? e * e : 0.f); }
#pragma unroll
        for (int m = 32; m >= 1; m >>= 1) sq += __shfl_xor(sq, m);
        float mbf = rbf(mean), den = rbf(sqrtf(rbf(rbf(sq * (1.0f / D_)) + 1e-5f)));
#pragma unroll
        for (int hf = 0; hf < 2; ++hf) {
          u32x4 o;
#pragma unroll
          for (int i = 0; i < 4; ++i) o[i] = pack2(rbf(v[hf * 8 + 2 * i] - mbf) / den, rbf(v[hf * 8 + 2 * i + 1] - mbf) / den);
          if (hf == 0 || two) st_sc1_16(rW, WS_HN + (u32)(row * D_ + (hf == 0 ? lane : 64 + lane) * 8) * 2, o);
        }
      }
      signal_flag(ws, WS_FLN + (u32)mb * 64);
    } else if (k < 99) {
      wait_flag(ws, WS_FLN + (u32)mb * 64, 11);
      acquire_bar();
      const FP p = getp();
      if (mb == 7) TL(128 + gb, 16);
      gtile<4, 3, E_GLU>((const u16*)(ws + WS_HN), D_, p.wg, p.wu, F_, 10, mb * 256, (k - 83) * 96, rW, WS_GU, F_, nullptr, nullptr, smem);
      signal_flag(ws, WS_FGL + (u32)mb * 64);
    } else if (k < 119) {
      wait_flag(ws, WS_FGL + (u32)mb * 64, 16);
      acquire_bar();
      const FP p = getp();
      if (mb == 7) TL(128 + gb, 16);
      gtile<2, 1, E_DOWN>((const u16*)(ws + WS_GU), F_, p.wd, nullptr, D_, 24, mb * 256 + ((k - 99) / 10) * 128, ((k - 99) % 10) * 64, rW, 0, D_, (const u16*)(ws + WS_H), p.out, smem);
      __syncthreads();
    }
    TL(128 + gb, 8 + mb);
  }
}

constexpr int MST = 656, QST = 80, VST = 208, PST = 72;
constexpr int CL_MEM = 0;
constexpr int CL_Q   = 8 * MST * 2;
constexpr int KBUF   = 48 * QST * 2;
constexpr int VBUF   = 40 * VST * 2;
constexpr int CL_K   = CL_Q + 8 * QST * 2;
constexpr int CL_V   = CL_K + 2 * KBUF;
constexpr int CL_P   = CL_V + 2 * VBUF;
constexpr int CL_F   = CL_P + 8 * PST * 2;
constexpr int CL_END = CL_F + 16;
constexpr int CL_STG = CL_Q;

#define STAMP(i) do {} while (0)

__device__ __forceinline__ void chain_role(int cu, char* smem) {
  const FP p = getp();
  const int h = cu >> 2, j = cu & 3;
  const int tid = threadIdx.x, lane = tid & 63;
  const int wave = __builtin_amdgcn_readfirstlane(tid >> 6);
  const int fr = lane & 15, g = lane >> 4, q4 = fr >> 2, p4 = fr & 3;
  char* ws = p.ws;
  u32* ctl = (u32*)(ws + WS_CTL);
  rsrc_t rW = __builtin_amdgcn_make_buffer_rsrc(ws, 0, WS_END, 0x00020000);
  const u32 a2base = WS_GA2 + (u32)(cu % A2_REP) * A2_STRIDE;

  TL(cu, 0);
  bf16x8 Wf[20];
  bf16x8 Wof[6];
  {
    u16* stg = (u16*)(smem + CL_STG);
#pragma unroll 1
    for (int mat = 0; mat < 3; ++mat) {
      const u16* W = mat == 0 ? p.Wq : (mat == 1 ? p.Wk : p.Wv);
      __syncthreads();
      for (int c = tid; c < 640 * 6; c += NTH) {
        int d = c / 6, ch = c % 6;
        *(u32x4*)(stg + d * 48 + ch * 8) = *(const u32x4*)(W + (size_t)d * HE_ + h * E_ + j * 48 + ch * 8);
      }
      __syncthreads();
      if (wave / 3 == mat) {
        int e0 = (wave % 3) * 16;
#pragma unroll
        for (int ks = 0; ks < 20; ++ks) {
          int r0 = ks * 32 + g * 8 + q4;
          Wf[ks] = cat8(tr_read(stg + r0 * 48 + e0 + p4 * 4), tr_read(stg + (r0 + 4) * 48 + e0 + p4 * 4));
        }
      }
    }
    __syncthreads();
    for (int c = tid; c < 192 * 20; c += NTH) {
      int e = c / 20, ch = c % 20;
      *(u32x4*)(stg + e * 160 + ch * 8) = *(const u32x4*)(p.Wo + (size_t)(h * E_ + e) * D_ + j * 160 + ch * 8);
    }
    __syncthreads();
    const int wc = wave < 10 ? wave : 0;
#pragma unroll
    for (int t = 0; t < 6; ++t) {
      int r0 = t * 32 + g * 8 + q4;
      Wof[t] = cat8(tr_read(stg + r0 * 160 + wc * 16 + p4 * 4), tr_read(stg + (r0 + 4) * 160 + wc * 16 + p4 * 4));
    }
    __syncthreads();
  }
  for (int i = tid; i < (CL_END - CL_Q) / 4; i += NTH) ((u32*)(smem + CL_Q))[i] = 0u;
  __syncthreads();

  u16* memL = (u16*)(smem + CL_MEM);
  u16* qL = (u16*)(smem + CL_Q);
  u16* pL = (u16*)(smem + CL_P);
  volatile LAS u32* vflag = (volatile LAS u32*)(smem + CL_F);
  volatile LAS u32* pflag = vflag + 1;
  bool dead = false;

  auto xfetch = [&](int seg, int bf, int ii, int ln) {
    u32 off;
    if (ii < 5) { int c = ii * 64 + ln; int tok = c / 10, ch = c % 10;
      off = ch < 6 ? WS_KX + (u32)((seg * SEG_ + tok) * HE_ + h * E_ + j * 48 + ch * 8) * 2 : 2048u;
      __builtin_amdgcn_global_load_lds((const u32*)(ws + off), (LAS u32*)(smem + CL_K + bf * KBUF + 8 * QST * 2 + ii * 1024), 16, 0, 16);
    } else { int c = (ii - 5) * 64 + ln; int tok = c / 26, ch = c % 26; if (ch >= 24) ch = 0;
      off = WS_VX + (u32)((seg * SEG_ + tok) * HE_ + h * E_ + ch * 8) * 2;
      __builtin_amdgcn_global_load_lds((const u32*)(ws + off), (LAS u32*)(smem + CL_V + bf * VBUF + 8 * VST * 2 + (ii - 5) * 1024), 16, 0, 16);
    }
  };
  auto wait_kv = [&](int mb) {
    u32* fk = (u32*)(ws + WS_FQKV + (u32)(mb * 96 + h) * 64); u32* fv = (u32*)(ws + WS_FQKV + (u32)(mb * 96 + 32 + h) * 64);
    u32 spins = 0;
    for (;;) {
      u32 a = __hip_atomic_load(fk, RLX_AGENT), b = __hip_atomic_load(fv, RLX_AGENT);
      if (__builtin_amdgcn_readfirstlane(a) != 0u && __builtin_amdgcn_readfirstlane(b) != 0u) break;
      __builtin_amdgcn_s_sleep(4);
      if (dead || spin_fail(spins, ctl)) { dead = true; break; }
    }
    asm volatile("" ::: "memory");
  };
  TL(cu, 1);
  wait_kv(0);
  TL(cu, 2);
  for (int ii = wave; ii < 18; ii += 12) xfetch(0, 0, ii, lane);
  asm volatile("s_waitcnt vmcnt(0)" ::: "memory");
  __syncthreads();
  u32 vwx[4];
  auto calc_vwx = [&](int bf, int fr_, int g_) {
    const u16* vb = (const u16*)(smem + CL_V + bf * VBUF) + (8 + fr_) * VST + g_ * 8;
    f32x4 a0 = {0.f, 0.f, 0.f, 0.f}, a1 = {0.f, 0.f, 0.f, 0.f};
#pragma unroll
    for (int t = 0; t < 6; ++t) {
      a0 = mfma16(*(const bf16x8*)(vb + t * 32), Wof[t], a0);
      a1 = mfma16(*(const bf16x8*)(vb + 16 * VST + t * 32), Wof[t], a1);
    }
    vwx[0] = pack2(a0[0], a0[1]); vwx[1] = pack2(a0[2], a0[3]); vwx[2] = pack2(a1[0], a1[1]); vwx[3] = pack2(a1[2], a1[3]);
  };
  calc_vwx(0, fr, g);

#pragma unroll 1
  for (int s = 0; s < NSEG_; ++s) {
    int tz = threadIdx.x; asm volatile("" : "+v"(tz));
    const int tid = tz, lane = tz & 63, fr = lane & 15, g = lane >> 4;
    const u32 ep = (u32)s + 1u;
    const u32 par = ep & 1u;
    const int buf = s & 1;
    u16* kL = (u16*)(smem + CL_K + buf * KBUF);
    u16* vL = (u16*)(smem + CL_V + buf * VBUF);
    STAMP(0);
    if (s == 0) {
      for (int c = tid; c < 8 * 80; c += NTH) { int r = c / 80, ch = c % 80; *(u32x4*)(memL + r * MST + ch * 8) = *(const u32x4*)(getp().mem0 + r * D_ + ch * 8); }
    } else if (wave < 10) {
      const u32 epm = (u32)s;
      u32x4 a0, a1; u32 spins = 0;
      for (;;) {
        asm volatile("" ::: "memory");
        a0 = ld_sc1_16(rW, a2base + tid * 16); a1 = ld_sc1_16(rW, a2base + (tid + 640) * 16);
        bool ok = a0[1] == epm && a0[3] == epm && a1[1] == epm && a1[3] == epm;
        if (__all(ok)) break;
        if (dead || spin_fail(spins, ctl)) { dead = true; break; }
      }
      { int u = tid; int row = ((u >> 4) & 1) * 4 + ((u >> 2) & 3), col = (u >> 5) * 16 + (u & 3) * 4; *(u32x2*)(memL + row * MST + col) = (u32x2){a0[0], a0[2]}; }
      { int u = tid + 640; int row = ((u >> 4) & 1) * 4 + ((u >> 2) & 3), col = (u >> 5) * 16 + (u & 3) * 4; *(u32x2*)(memL + row * MST + col) = (u32x2){a1[0], a1[2]}; }
    }
    STAMP(1);
    lbar();
    STAMP(2);
    if (wave < 9) {
      f32x4 acc = {0.f, 0.f, 0.f, 0.f};
      const u16* mb = memL + (fr & 7) * MST + g * 8;
      bf16x8 b0[4], b1[4];
#pragma unroll
      for (int i = 0; i < 4; ++i) b0[i] = *(const bf16x8*)(mb + i * 32);
#pragma unroll
      for (int gq = 0; gq < 5; ++gq) {
        if (gq & 1) {
          if (gq < 4) {
#pragma unroll
            for (int i = 0; i < 4; ++i) b0[i] = *(const bf16x8*)(mb + ((gq + 1) * 4 + i) * 32);
          }
#pragma unroll
          for (int i = 0; i < 4; ++i) acc = mfma16(Wf[gq * 4 + i], b1[i], acc);
        } else {
          if (gq < 4) {
#pragma unroll
            for (int i = 0; i < 4; ++i) b1[i] = *(const bf16x8*)(mb + ((gq + 1) * 4 + i) * 32);
          }
#pragma unroll
          for (int i = 0; i < 4; ++i) acc = mfma16(Wf[gq * 4 + i], b0[i], acc);
        }
        __builtin_amdgcn_sched_barrier(0);
      }
      u32x2 w = {pack2(acc[0], acc[1]), pack2(acc[2], acc[3])};
      int e = (wave % 3) * 16 + g * 4;
      if (fr < 8) {
        if (wave < 3) *(u32x2*)(qL + fr * QST + e) = w;
        else if (wave < 6) {
          *(u32x2*)(kL + fr * QST + e) = w;
          st_sc1_8(rW, WS_KM + (u32)((s * M_ + fr) * HE_ + h * E_ + j * 48 + e) * 2, w);
        } else {
          st_sc1_16(rW, WS_GB + (u32)(cu * 192 + 96 + (wave - 6) * 32 + fr * 4 + g) * 16, (u32x4){w[0], ep, w[1], ep});
          st_sc1_8(rW, WS_VM + (u32)((s * M_ + fr) * HE_ + h * E_ + j * 48 + e) * 2, w);
        }
      }
    }
    if (s == NSEG_ - 1) break;
    lbar();
    STAMP(3);
    if (wave < 3) {
      f32x4 acc = {0.f, 0.f, 0.f, 0.f};
#pragma unroll
      for (int kk = 0; kk < 2; ++kk) {
        bf16x8 a = *(const bf16x8*)(kL + (wave * 16 + fr) * QST + kk * 32 + g * 8);
        bf16x8 b = *(const bf16x8*)(qL + (fr & 7) * QST + kk * 32 + g * 8);
        acc = mfma16(a, b, acc);
      }
      if (fr < 8)
        st_sc1_16(rW, WS_GB + (u32)(cu * 192 + wave * 32 + fr * 4 + g) * 16,
                  (u32x4){(__float_as_uint(acc[0]) & ~1u) | par, (__float_as_uint(acc[1]) & ~1u) | par,
                          (__float_as_uint(acc[2]) & ~1u) | par, (__float_as_uint(acc[3]) & ~1u) | par});
    }
    STAMP(4);
    if (wave < 9) {
      if (((s + 1) & 7) == 0) wait_kv((s + 1) >> 3);
      xfetch(s + 1, buf ^ 1, wave, lane); xfetch(s + 1, buf ^ 1, wave + 9, lane);
    }
    if (wave >= 10) {
      const int r = lane & 7, q = lane >> 3;
      const u32 o0 = WS_GB + (u32)(h * 4 * 192 + (q >> 2) * 32 + r * 4 + (q & 3)) * 16;
      const u32 o1 = WS_GB + (u32)(h * 4 * 192 + 64 + r * 4 + (q & 1)) * 16;
      u32x4 a[4], b[4]; u32 spins = 0;
      for (;;) {
        asm volatile("" ::: "memory");
        u32 aa = 0xffffffffu, oo = 0u;
#pragma unroll
        for (int i = 0; i < 4; ++i) {
          a[i] = __builtin_amdgcn_raw_buffer_load_b128(rW, o0, i * 3072, 16);
          b[i] = __builtin_amdgcn_raw_buffer_load_b128(rW, o1, i * 3072, 16);
        }
#pragma unroll
        for (int i = 0; i < 4; ++i) {
          aa &= a[i][0] & a[i][1] & a[i][2] & a[i][3] & b[i][0] & b[i][1] & b[i][2] & b[i][3];
          oo |= a[i][0] | a[i][1] | a[i][2] | a[i][3] | b[i][0] | b[i][1] | b[i][2] | b[i][3];
        }
        bool ok = (aa & 1u) == par && (oo & 1u) == par;
        if (__all(ok)) break;
        if (dead || spin_fail(spins, ctl)) { dead = true; break; }
      }
      STAMP(5);
      const float scale = 0.07216878364870322f;
      float sc[8];
#pragma unroll
      for (int k = 0; k < 4; ++k) {
        float x = (__uint_as_float(a[0][k]) + __uint_as_float(a[1][k])) + (__uint_as_float(a[2][k]) + __uint_as_float(a[3][k]));
        float y = (__uint_as_float(b[0][k]) + __uint_as_float(b[1][k])) + (__uint_as_float(b[2][k]) + __uint_as_float(b[3][k]));
        sc[k] = rbf(rbf(x) * scale); sc[4 + k] = rbf(rbf(y) * scale);
      }
      float mx = fmaxf(fmaxf(fmaxf(sc[0], sc[1]), fmaxf(sc[2], sc[3])), fmaxf(fmaxf(sc[4], sc[5]), fmaxf(sc[6], sc[7])));
      mx = fmaxf(mx, __uint_as_float(__builtin_amdgcn_update_dpp(0, __float_as_uint(mx), 0x128, 0xf, 0xf, false)));
      mx = red16_max(mx); mx = red32_max(mx);
      float sum = 0.f, sum2 = 0.f;
#pragma unroll
      for (int k = 0; k < 4; ++k) { sc[k] = __expf(sc[k] - mx); sum += sc[k]; sc[4 + k] = __expf(sc[4 + k] - mx); sum2 += sc[4 + k]; }
      if (q < 2) sum += sum2;
      sum += __uint_as_float(__builtin_amdgcn_update_dpp(0, __float_as_uint(sum), 0x128, 0xf, 0xf, false));
      sum = red16_add(sum); sum = red32_add(sum);
      float inv = __builtin_amdgcn_rcpf(sum);
      *(u32x2*)(pL + r * PST + q * 4) = (u32x2){pack2(sc[0] * inv, sc[1] * inv), pack2(sc[2] * inv, sc[3] * inv)};
      if (q < 2) *(u32x2*)(pL + r * PST + 32 + q * 4) = (u32x2){pack2(sc[4] * inv, sc[5] * inv), pack2(sc[6] * inv, sc[7] * inv)};
      asm volatile("s_waitcnt lgkmcnt(0)" ::: "memory");
      if (lane == 0) *pflag = ep;
      STAMP(6);
    } else if (wave == 9) {
      u32x4 v[6]; u32 spins = 0;
      for (;;) {
        asm volatile("" ::: "memory");
        bool ok = true;
#pragma unroll
        for (int i = 0; i < 6; ++i) { int id = lane + 64 * i; int src = id / 96, u = id % 96; v[i] = ld_sc1_16(rW, WS_GB + (u32)((h * 4 + src) * 192 + 96 + u) * 16); }
#pragma unroll
        for (int i = 0; i < 6; ++i) ok = ok && v[i][1] == ep && v[i][3] == ep;
        if (__all(ok)) break;
        if (dead || spin_fail(spins, ctl)) { dead = true; break; }
      }
#pragma unroll
      for (int i = 0; i < 6; ++i) {
        int id = lane + 64 * i; int src = id / 96, u = id % 96; int vt = u >> 5, rr = (u >> 2) & 7, gg = u & 3;
        *(u32x2*)(vL + rr * VST + src * 48 + vt * 16 + gg * 4) = (u32x2){v[i][0], v[i][2]};
      }
      asm volatile("s_waitcnt lgkmcnt(0)" ::: "memory");
      if (lane == 0) *vflag = ep;
    }
    if (wave < 10) {
      while (*vflag != ep) { if (dead) break; }
      asm volatile("" ::: "memory");
      f32x4 am = {0.f, 0.f, 0.f, 0.f};
      const u16* vb = vL + (fr & 7) * VST + g * 8;
#pragma unroll
      for (int t = 0; t < 6; ++t) am = mfma16(*(const bf16x8*)(vb + t * 32), Wof[t], am);
      u32x4 a0u = {vwx[0], vwx[1], vwx[2], vwx[3]};
      u32x4 a1u = {g < 2 ? pack2(am[0], am[1]) : 0u, g < 2 ? pack2(am[2], am[3]) : 0u, 0u, 0u};
      while (*pflag != ep) { if (dead) break; }
      asm volatile("s_waitcnt vmcnt(0)" ::: "memory");
      const u16* pr = pL + (fr & 7) * PST;
      u32x2 x0 = *(const u32x2*)(pr + 8 + g * 4), x1 = *(const u32x2*)(pr + 24 + g * 4), x2 = *(const u32x2*)(pr + (g & 1) * 4);
      u32x4 b0u = {x0[0], x0[1], x1[0], x1[1]};
      u32x4 b1u = {g < 2 ? x2[0] : 0u, g < 2 ? x2[1] : 0u, 0u, 0u};
      f32x4 acc = {0.f, 0.f, 0.f, 0.f};
      acc = mfma16(__builtin_bit_cast(bf16x8, a0u), __builtin_bit_cast(bf16x8, b0u), acc);
      acc = mfma16(__builtin_bit_cast(bf16x8, a1u), __builtin_bit_cast(bf16x8, b1u), acc);
      if (fr < 8) {
        u32 o = WS_GA1 + (u32)(((j * 10 + wave) * 32 + h) * 32 + fr * 4 + g) * 16;
        st_sc1_16(rW, o, (u32x4){(__float_as_uint(acc[0]) & ~1u) | par, (__float_as_uint(acc[1]) & ~1u) | par,
                                 (__float_as_uint(acc[2]) & ~1u) | par, (__float_as_uint(acc[3]) & ~1u) | par});
      }
    }
    STAMP(8);
    lbar();
    if ((s & 7) == 7) TL(cu, 3 + (s >> 3));
    if ((s & 7) == 7 && tid == 0) __hip_atomic_store((u32*)(ws + WS_FCH + (u32)cu * 64), (u32)(s >> 3) + 1u, RLX_AGENT);
    if (wave < 10) calc_vwx(buf ^ 1, fr, g);
    if (wave >= 10) {
      if (cu < 80) {
        const int ch = lane & 15, hq = lane >> 4;
        const u32 rbase = WS_GA1 + (u32)(((cu >> 1) * 32 + hq * 8) * 32 + (cu & 1) * 16 + ch) * 16;
        u32x4 a[8]; u32 spins = 0;
        for (;;) {
          asm volatile("" ::: "memory");
          u32 aa = 0xffffffffu, oo = 0u;
#pragma unroll
          for (int i = 0; i < 8; ++i) a[i] = __builtin_amdgcn_raw_buffer_load_b128(rW, rbase, i * 512, 16);
#pragma unroll
          for (int i = 0; i < 8; ++i) { aa &= a[i][0] & a[i][1] & a[i][2] & a[i][3]; oo |= a[i][0] | a[i][1] | a[i][2] | a[i][3]; }
          bool ok = (aa & 1u) == par && (oo & 1u) == par;
          if (__all(ok)) break;
          if (dead || spin_fail(spins, ctl)) { dead = true; break; }
        }
        STAMP(9);
        float t[4];
#pragma unroll
        for (int k = 0; k < 4; ++k) {
          float x = ((__uint_as_float(a[0][k]) + __uint_as_float(a[1][k])) + (__uint_as_float(a[2][k]) + __uint_as_float(a[3][k])))
                  + ((__uint_as_float(a[4][k]) + __uint_as_float(a[5][k])) + (__uint_as_float(a[6][k]) + __uint_as_float(a[7][k])));
          x = red16_add(x); x = red32_add(x); t[k] = x;
        }
        u32 w0 = pack2(t[0], t[1]), w1 = pack2(t[2], t[3]);
#pragma unroll
        for (int it = 0; it < 4; ++it) st_sc1_16(rW, WS_GA2 + (u32)(cu * 16 + ch) * 16 + (u32)(hq + 4 * it) * A2_STRIDE, (u32x4){w0, ep, w1, ep});
        STAMP(10);
      } else if (wave == 10) {
        u32 spins = 0;
        for (;;) {
          asm volatile("" ::: "memory");
          u32x4 a = ld_sc1_16(rW, a2base + (u32)(cu - 80) * 256);
          if (__all(a[1] == ep)) break;
          if (dead || spin_fail(spins, ctl)) { dead = true; break; }
        }
      }
    }
    lbar();
  }
  TL(cu, 10);
  asm volatile("s_waitcnt vmcnt(0)" ::: "memory");
  __syncthreads();
  if (tid == 0) __hip_atomic_store((u32*)(ws + WS_FCH + (u32)cu * 64), 8u, RLX_AGENT);
}

__global__ __launch_bounds__(NTH, 3) void k_main(FP p) {
  extern __shared__ __attribute__((aligned(16))) char smem[];
  const int b = blockIdx.x;
  const int id = (b >> 4) * 8 + (b & 7);
  if ((b >> 3) & 1) gemm_role(id, smem);
  else chain_role(id, smem);
}

extern "C" void kernel_launch(void* const* d_in, const int* in_sizes, int n_in,
                              void* d_out, int out_size, void* d_ws, size_t ws_size,
                              hipStream_t stream) {
  char* ws = (char*)d_ws;
  static bool attr_done = false;
  if (!attr_done) {
    (void)hipFuncSetAttribute((const void*)k_main, hipFuncAttributeMaxDynamicSharedMemorySize, LDS_BYTES);
    attr_done = true;
  }
  (void)hipMemsetAsync(ws, 0, WS_ZEND, stream);
  k_ln<<<S_ / 4, 256, 0, stream>>>((const u16*)d_in[0], (u16*)(ws + WS_XN));
  FP p{(const u16*)d_in[0], (const u16*)d_in[1], (const u16*)d_in[2], (const u16*)d_in[3], (const u16*)d_in[4], (const u16*)d_in[5],
       (const u16*)d_in[8], (const u16*)d_in[9], (const u16*)d_in[10], (u16*)d_out, ws};
  k_main<<<256, NTH, LDS_BYTES, stream>>>(p);
}
```

```cpp
#include <hip/hip_runtime.h>
#include <hip/hip_bf16.h>
#include <stdint.h>

typedef unsigned short u16;
typedef unsigned int u32;
typedef unsigned long long u64;
typedef short bf16x8 __attribute__((ext_vector_type(8)));
typedef short bf16x4 __attribute__((ext_vector_type(4)));
typedef float f32x4 __attribute__((ext_vector_type(4)));
typedef u32 u32x4 __attribute__((ext_vector_type(4)));
typedef u32 u32x2 __attribute__((ext_vector_type(2)));
#define LAS __attribute__((address_space(3)))
#define RLX_AGENT __ATOMIC_RELAXED, __HIP_MEMORY_SCOPE_AGENT

constexpr int S_ = 2048, D_ = 640, H_ = 32, E_ = 192, HE_ = H_ * E_;
constexpr int M_ = 8, SEG_ = 32, NSEG_ = 64, F_ = 1536;

constexpr u32 WS_CTL   = 0;
constexpr u32 WS_FQKV  = 4096;
constexpr u32 WS_FCH   = WS_FQKV + 8 * 96 * 64;
constexpr u32 WS_FAT   = WS_FCH + 128 * 64;
constexpr u32 WS_FOP   = WS_FAT + 64 * 64;
constexpr u32 WS_FLN   = WS_FOP + 8 * 64;
constexpr u32 WS_FGL   = WS_FLN + 8 * 64;
constexpr u32 WS_GA2   = (WS_FGL + 8 * 64 + 4095) / 4096 * 4096;
constexpr int A2_REP = 16, A2_STRIDE = 20736;
constexpr u32 WS_GB    = WS_GA2 + A2_REP * A2_STRIDE;
constexpr u32 WS_GA1   = WS_GB + 128 * 3072;
constexpr u32 WS_ZEND  = WS_GA1 + 40 * 32 * 512;
constexpr u32 WS_XN    = (WS_ZEND + 4095) / 4096 * 4096;
constexpr u32 WS_KX    = WS_XN + S_ * D_ * 2;
constexpr u32 WS_VX    = WS_KX + S_ * HE_ * 2;
constexpr u32 WS_QX    = WS_VX + S_ * HE_ * 2;
constexpr u32 WS_KM    = WS_QX + S_ * HE_ * 2;
constexpr u32 WS_VM    = WS_KM + NSEG_ * M_ * HE_ * 2;
constexpr u32 WS_OX    = WS_VM + NSEG_ * M_ * HE_ * 2;
constexpr u32 WS_H     = WS_OX + S_ * HE_ * 2;
constexpr u32 WS_HN    = WS_H + S_ * D_ * 2;
constexpr u32 WS_GU    = WS_HN + S_ * D_ * 2;
constexpr u32 WS_SLAB  = WS_GU + S_ * F_ * 2;
constexpr int NSLAB = 12;
constexpr u32 WS_END   = WS_SLAB + (u32)NSLAB * S_ * D_ * 4;

__device__ __forceinline__ float bf2f(u16 v) { return __uint_as_float(((u32)v) << 16); }
__device__ __forceinline__ u16 f2bf(float f) {
  __hip_bfloat16 b = __float2bfloat16(f);
  return *reinterpret_cast<u16*>(&b);
}
__device__ __forceinline__ float rbf(float f) { return bf2f(f2bf(f)); }
__device__ __forceinline__ u32 pack2(float a, float b) { return (u32)f2bf(a) | ((u32)f2bf(b) << 16); }
__device__ __forceinline__ f32x4 mfma16(bf16x8 a, bf16x8 b, f32x4 c) {
  return __builtin_amdgcn_mfma_f32_16x16x32_bf16(a, b, c, 0, 0, 0);
}
__device__ __forceinline__ bf16x4 tr_read(const void* p) {
  return __builtin_amdgcn_ds_read_tr16_b64_v4i16((LAS bf16x4*)p);
}
__device__ __forceinline__ bf16x8 cat8(bf16x4 a, bf16x4 b) {
  bf16x8 r; r[0]=a[0]; r[1]=a[1]; r[2]=a[2]; r[3]=a[3]; r[4]=b[0]; r[5]=b[1]; r[6]=b[2]; r[7]=b[3]; return r;
}
__device__ __forceinline__ void lbar() { asm volatile("s_waitcnt lgkmcnt(0)\n\ts_barrier" ::: "memory"); }
typedef __amdgpu_buffer_rsrc_t rsrc_t;
__device__ __forceinline__ u32x4 ld_sc1_16(rsrc_t r, u32 off) { return __builtin_amdgcn_raw_buffer_load_b128(r, off, 0, 16); }
__device__ __forceinline__ void st_sc1_16(rsrc_t r, u32 off, u32x4 v) { __builtin_amdgcn_raw_buffer_store_b128(v, r, off, 0, 16); }
__device__ __forceinline__ void st_sc1_8(rsrc_t r, u32 off, u32x2 v) { __builtin_amdgcn_raw_buffer_store_b64(v, r, off, 0, 16); }
__device__ __forceinline__ float red16_max(float x) {
  auto r = __builtin_amdgcn_permlane16_swap(__float_as_uint(x), __float_as_uint(x), false, false);
  return fmaxf(__uint_as_float(r[0]), __uint_as_float(r[1]));
}
__device__ __forceinline__ float red32_max(float x) {
  auto r = __builtin_amdgcn_permlane32_swap(__float_as_uint(x), __float_as_uint(x), false, false);
  return fmaxf(__uint_as_float(r[0]), __uint_as_float(r[1]));
}
__device__ __forceinline__ float red16_add(float x) {
  auto r = __builtin_amdgcn_permlane16_swap(__float_as_uint(x), __float_as_uint(x), false, false);
  return __uint_as_float(r[0]) + __uint_as_float(r[1]);
}
__device__ __forceinline__ float red32_add(float x) {
  auto r = __builtin_amdgcn_permlane32_swap(__float_as_uint(x), __float_as_uint(x), false, false);
  return __uint_as_float(r[0]) + __uint_as_float(r[1]);
}

constexpr u32 SPIN_CAP = 1u << 18;
__device__ __forceinline__ bool spin_fail(u32& spins, u32* ctl) {
  ++spins;
  if (spins > SPIN_CAP) { __hip_atomic_store(ctl, 1u, RLX_AGENT); return true; }
  return (spins & 255u) == 0u && __builtin_amdgcn_readfirstlane(__hip_atomic_load(ctl, RLX_AGENT)) != 0u;
}

struct FP {
  const u16* x; const u16* mem0; const u16* Wq; const u16* Wk; const u16* Wv; const u16* Wo;
  const u16* wg; const u16* wu; const u16* wd; u16* out; char* ws;
};
typedef const __attribute__((address_space(4))) FP* kargp_t;
__device__ __forceinline__ FP getp() {
#if defined(__HIP_DEVICE_COMPILE__)
  kargp_t kp = (kargp_t)__builtin_amdgcn_kernarg_segment_ptr(); asm volatile("" : "+s"(kp)); return *kp;
#else
  return FP{};
#endif
}
constexpr int NTH = 768;
constexpr int LDS_BYTES = 153600;

__device__ __forceinline__ void ln_row(const float (&v)[10], u32 (&o)[5]) {
  float sum = 0.f;
#pragma unroll
  for (int i = 0; i < 10; ++i) sum += v[i];
#pragma unroll
  for (int m = 32; m >= 1; m >>= 1) sum += __shfl_xor(sum, m);
  float mean = sum * (1.0f / D_);
  float sq = 0.f;
#pragma unroll
  for (int i = 0; i < 10; ++i) { float d = v[i] - mean; sq += d * d; }
#pragma unroll
  for (int m = 32; m >= 1; m >>= 1) sq += __shfl_xor(sq, m);
  float var = sq * (1.0f / D_);
  float mb = rbf(mean), vb = rbf(var);
  float den = rbf(sqrtf(rbf(vb + 1e-5f)));
#pragma unroll
  for (int i = 0; i < 5; ++i) o[i] = pack2(rbf(v[2 * i] - mb) / den, rbf(v[2 * i + 1] - mb) / den);
}
__global__ __launch_bounds__(256) void k_ln(const u16* __restrict__ x, u16* __restrict__ y) {
  int row = blockIdx.x * 4 + (threadIdx.x >> 6);
  int lane = threadIdx.x & 63;
  const u32* xr = (const u32*)(x + (size_t)row * D_);
  float v[10];
#pragma unroll
  for (int i = 0; i < 5; ++i) { u32 w = xr[i * 64 + lane]; v[2 * i] = bf2f((u16)(w & 0xffff)); v[2 * i + 1] = bf2f((u16)(w >> 16)); }
  u32 o[5]; ln_row(v, o);
  u32* yr = (u32*)(y + (size_t)row * D_);
#pragma unroll
  for (int i = 0; i < 5; ++i) yr[i * 64 + lane] = o[i];
}

__device__ __forceinline__ void wait_flag(char* ws, u32 foff, u32 target) {
  if (threadIdx.x == 0) {
    u32* f = (u32*)(ws + foff); u32* ctl = (u32*)(ws + WS_CTL); u32 spins = 0;
    while (__hip_atomic_load(f, RLX_AGENT) < target) {
      __builtin_amdgcn_s_sleep(8);
      if (++spins > (1u << 20)) { __hip_atomic_store(ctl, 1u, RLX_AGENT); break; }
      if ((spins & 63u) == 0u && __hip_atomic_load(ctl, RLX_AGENT) != 0u) break;
    }
  }
}
__device__ __forceinline__ void acquire_bar() {
  if (threadIdx.x == 0) { __builtin_amdgcn_fence(__ATOMIC_ACQUIRE, "agent"); asm volatile("s_waitcnt vmcnt(0)" ::: "memory"); }
  __syncthreads();
}
__device__ __forceinline__ void signal_flag(char* ws, u32 foff) {
  asm volatile("s_waitcnt vmcnt(0)" ::: "memory");
  __syncthreads();
  if (threadIdx.x == 0) __hip_atomic_fetch_add((u32*)(ws + foff), 1u, RLX_AGENT);
}

#define TLMB 3
#define TL(blk, i) do {} while (0)
template <int NWN> __device__ __forceinline__ int bswz(int row) {
  if (NWN == 3) return (((row >> 1) & 1) | (((row >> 3) & 1) << 1)) << 1;
  else if (NWN == 2) return ((row & 3) << 2) | ((row >> 2) & 3);
  else return (((row >> 1) & 1) | (((row >> 3) & 1) << 1)) << 1;
}
enum { E_BF16 = 0, E_SLAB = 1, E_GLU = 2, E_DOWN = 3 };
template <int NWM, int NWN, int KG, int EPI>
__device__ __forceinline__ void gtile(const u16* __restrict__ A, int lda, const u16* __restrict__ B0, const u16* __restrict__ B1, int ldb,
                                      int nk, int m0, int nb, rsrc_t rW, u32 coff, int ldc, const u16* resid, u16* outp, char* smem) {
  constexpr int BM = NWM * 64, BN = NWN * 64, BROW = BN * 2, NA = BM / 16, NI = NA + BN / 16, NT = NI * KG, ST_BYTES = NT * 1024;
  constexpr int NST = (143360 / ST_BYTES) < 16 ? (143360 / ST_BYTES) : 16, PD = NST - 1;
  constexpr int NPW = (NT + 11) / 12;
  constexpr int NW = NWM * NWN;
  int tz = threadIdx.x; asm volatile("" : "+v"(tz));
  const int tid = tz, lane = tid & 63, wave = __builtin_amdgcn_readfirstlane(tid >> 6);
  const int kg = wave / NW, wv = wave % NW, wm = wv % NWM, wn = wv / NWM;
  const int fr = lane & 15, g = lane >> 4;
  const u16* src[NPW]; u32 sstep[NPW];
#pragma unroll
  for (int i = 0; i < NPW; ++i) {
    int it = wave + 12 * i; if (it >= NT) it = 0;
    int sub = it / NI, ii = it % NI;
    if (ii < NA) { int b = ii * 1024 + lane * 16; int row = b >> 6, pos = (b >> 4) & 3, ch = pos ^ ((4 - (row >> 2)) & 3);
      src[i] = A + (size_t)(m0 + row) * lda + ch * 8 + sub * 32; sstep[i] = 32 * KG; }
    else { int b = (ii - NA) * 1024 + lane * 16; int row = b / BROW, pos = (b % BROW) >> 4;
      int ch = pos ^ bswz<NWN>(row);
      if (EPI == E_GLU) { int w2 = ch >> 3, within = ch & 7; const u16* base = (within >> 2) ? B1 : B0;
        src[i] = base + (size_t)(row + sub * 32) * ldb + nb + w2 * 32 + (within & 3) * 8; }
      else src[i] = B0 + (size_t)(row + sub * 32) * ldb + nb + ch * 8;
      sstep[i] = 32u * KG * (u32)ldb; }
  }
  const int npw = (wave + 12 * (NPW - 1)) < NT ? NPW : NPW - 1;
  auto stage = [&](int kt, int buf) {
    char* sb = smem + buf * ST_BYTES;
#pragma unroll
    for (int i = 0; i < NPW; ++i)
      if (i < npw) __builtin_amdgcn_global_load_lds((const u32*)(src[i] + (size_t)kt * sstep[i]), (LAS u32*)(sb + (wave + 12 * i) * 1024), 16, 0, 0);
  };
  f32x4 acc[4][4];
#pragma unroll
  for (int a = 0; a < 4; ++a)
#pragma unroll
    for (int b = 0; b < 4; ++b) acc[a][b] = (f32x4){0.f, 0.f, 0.f, 0.f};
  const bool compk = wave < NW * KG;
  const bool comp = wave < NW;
  const int nst = nk * 2 / KG;
#pragma unroll 1
  for (int i = 0; i < PD; ++i) if (i < nst) stage(i, i);
  int cb = 0, pb = PD;
  const int q = fr >> 2, pp = fr & 3;
  const int r0 = g * 8 + q, r1 = r0 + 4;
  const int x0 = bswz<NWN>(r0), x1 = bswz<NWN>(r1);
  const int aoff = kg * NI * 1024 + (wm * 64 + fr) * 64 + ((g ^ ((4 - (fr >> 2)) & 3)) << 4);
#pragma unroll 1
  for (int kt = 0; kt < nst; ++kt) {
    if (kt + PD <= nst) {
      if (npw == NPW) asm volatile("s_waitcnt vmcnt(%0)" :: "i"(NPW * (PD - 1)) : "memory");
      else asm volatile("s_waitcnt vmcnt(%0)" :: "i"((NPW - 1) * (PD - 1)) : "memory");
    }
    else asm volatile("s_waitcnt vmcnt(0)" ::: "memory");
    lbar();
    if (kt + PD < nst) stage(kt + PD, pb);
    if (compk) {
      const char* sa = smem + cb * ST_BYTES; const char* sb = sa + (kg * NI + NA) * 1024;
      bf16x8 af[4], bfr[4];
#pragma unroll
      for (int a = 0; a < 4; ++a) af[a] = *(const bf16x8*)(sa + aoff + a * 1024);
#pragma unroll
      for (int b = 0; b < 4; ++b) {
        int ch = wn * 8 + b * 2 + (pp >> 1);
        bfr[b] = cat8(tr_read(sb + r0 * BROW + ((ch ^ x0) << 4) + 8 * (pp & 1)), tr_read(sb + r1 * BROW + ((ch ^ x1) << 4) + 8 * (pp & 1)));
      }
#pragma unroll
      for (int a = 0; a < 4; ++a)
#pragma unroll
        for (int b = 0; b < 4; ++b) acc[a][b] = mfma16(bfr[b], af[a], acc[a][b]);
    }
    cb = cb == NST - 1 ? 0 : cb + 1; pb = pb == NST - 1 ? 0 : pb + 1;
  }
  if (KG > 1) {
    lbar();
    if (compk && kg > 0) {
      f32x4* d = (f32x4*)(smem + (wave - NW) * 16384) + lane;
#pragma unroll
      for (int a = 0; a < 4; ++a)
#pragma unroll
        for (int b = 0; b < 4; ++b) d[(a * 4 + b) * 64] = acc[a][b];
    }
    lbar();
    if (comp) {
#pragma unroll
      for (int k2 = 1; k2 < KG; ++k2) {
        const f32x4* d = (const f32x4*)(smem + ((k2 - 1) * NW + wv) * 16384) + lane;
#pragma unroll
        for (int a = 0; a < 4; ++a)
#pragma unroll
          for (int b = 0; b < 4; ++b) acc[a][b] += d[(a * 4 + b) * 64];
      }
    }
  }
  if (EPI == E_DOWN) {
    if (comp) {
#pragma unroll
      for (int a = 0; a < 4; ++a) {
        const int m = m0 + wm * 64 + a * 16 + fr;
#pragma unroll
        for (int b = 0; b < 4; ++b) {
          int n = nb + wn * 64 + b * 16 + g * 4;
          u32x2 rv = *(const u32x2*)(resid + (size_t)m * ldc + n);
          float o0 = rbf(acc[a][b][0]) + bf2f((u16)(rv[0] & 0xffff)), o1 = rbf(acc[a][b][1]) + bf2f((u16)(rv[0] >> 16));
          float o2 = rbf(acc[a][b][2]) + bf2f((u16)(rv[1] & 0xffff)), o3 = rbf(acc[a][b][3]) + bf2f((u16)(rv[1] >> 16));
          *(u32x2*)(outp + (size_t)m * ldc + n) = (u32x2){pack2(o0, o1), pack2(o2, o3)};
        }
      }
    }
  } else {
    constexpr int OC = EPI == E_GLU ? NWN * 32 : BN;
    constexpr int ES = EPI == E_SLAB ? 4 : 2;
    constexpr int RB = OC * ES, RS = RB + 16, CPR = RB / 16;
    lbar();
    if (comp) {
#pragma unroll
      for (int a = 0; a < 4; ++a) {
        char* rowp = smem + (wm * 64 + a * 16 + fr) * RS;
        if (EPI == E_GLU) {
#pragma unroll
          for (int b = 0; b < 2; ++b) {
            float o[4];
#pragma unroll
            for (int r = 0; r < 4; ++r) o[r] = rbf(acc[a][b][r]) * rbf(acc[a][b + 2][r]);
            *(u32x2*)(rowp + (wn * 32 + b * 16 + g * 4) * 2) = (u32x2){pack2(o[0], o[1]), pack2(o[2], o[3])};
          }
        } else {
#pragma unroll
          for (int b = 0; b < 4; ++b) {
            if (EPI == E_BF16) *(u32x2*)(rowp + (wn * 64 + b * 16 + g * 4) * 2) = (u32x2){pack2(acc[a][b][0], acc[a][b][1]), pack2(acc[a][b][2], acc[a][b][3])};
            else *(f32x4*)(rowp + (wn * 64 + b * 16 + g * 4) * 4) = acc[a][b];
          }
        }
      }
    }
    lbar();
#pragma unroll 2
    for (int c = tid; c < BM * CPR; c += NTH) {
      int row = c / CPR, ch = c % CPR;
      u32x4 v = *(const u32x4*)(smem + row * RS + ch * 16);
      st_sc1_16(rW, coff + (u32)((m0 + row) * ldc + nb) * ES + ch * 16, v);
    }
  }
}

__device__ __forceinline__ void attn_wave(const char* ws, rsrc_t rW, int s, int h, u16* vL) {
  int tz = threadIdx.x; asm volatile("" : "+v"(tz));
  const int lane = tz & 63, fr = lane & 15, g = lane >> 4, q4 = fr >> 2, p4 = fr & 3;
  const u16* qx = (const u16*)(ws + WS_QX); const u16* kx = (const u16*)(ws + WS_KX); const u16* vx = (const u16*)(ws + WS_VX);
  const u16* km = (const u16*)(ws + WS_KM); const u16* vm = (const u16*)(ws + WS_VM);
#pragma unroll
  for (int i = 0; i < 16; ++i) {
    int c = i * 64 + lane; int row = c / 25, ch = c % 25; if (row > 39) row = 39; if (ch > 23) ch = 23;
    const u16* sp = row < 8 ? vm + ((size_t)(s * M_ + row)) * HE_ : vx + ((size_t)(s * SEG_ + row - 8)) * HE_;
    __builtin_amdgcn_global_load_lds((const u32*)(sp + h * E_ + ch * 8), (LAS u32*)((char*)vL + i * 1024), 16, 0, 0);
  }
  f32x4 sc[3][2];
#pragma unroll
  for (int a = 0; a < 3; ++a) { sc[a][0] = (f32x4){0.f, 0.f, 0.f, 0.f}; sc[a][1] = (f32x4){0.f, 0.f, 0.f, 0.f}; }
  {
    const u16* kp[3];
#pragma unroll
    for (int a = 0; a < 3; ++a) {
      int key = a * 16 + fr; if (key > 39) key = 39;
      kp[a] = (key < 8 ? km + ((size_t)(s * M_ + key)) * HE_ : kx + ((size_t)(s * SEG_ + key - 8)) * HE_) + h * E_ + g * 8;
    }
    const u16* qp0 = qx + ((size_t)(s * SEG_ + fr)) * HE_ + h * E_ + g * 8;
    const u16* qp1 = qp0 + (size_t)16 * HE_;
#pragma unroll
    for (int hf = 0; hf < 2; ++hf) {
      bf16x8 qf[3][2], kf[3][3];
#pragma unroll
      for (int kk = 0; kk < 3; ++kk) {
        qf[kk][0] = *(const bf16x8*)(qp0 + (hf * 3 + kk) * 32); qf[kk][1] = *(const bf16x8*)(qp1 + (hf * 3 + kk) * 32);
#pragma unroll
        for (int a = 0; a < 3; ++a) kf[kk][a] = *(const bf16x8*)(kp[a] + (hf * 3 + kk) * 32);
      }
#pragma unroll
      for (int kk = 0; kk < 3; ++kk)
#pragma unroll
        for (int a = 0; a < 3; ++a) { sc[a][0] = mfma16(kf[kk][a], qf[kk][0], sc[a][0]); sc[a][1] = mfma16(kf[kk][a], qf[kk][1], sc[a][1]); }
    }
  }
  const float scale = 0.07216878364870322f;
  bf16x8 pb[2][2];
#pragma unroll
  for (int qt = 0; qt < 2; ++qt) {
    int qrow = qt * 16 + fr;
    float v[12]; float mx = -3.0e38f;
#pragma unroll
    for (int a = 0; a < 3; ++a)
#pragma unroll
      for (int r = 0; r < 4; ++r) {
        int key = a * 16 + g * 4 + r;
        float x = rbf(rbf(sc[a][qt][r]) * scale);
        bool vis = key < 8 || (key - 8) <= qrow;
        x = vis ? x : rbf(-1000000000.0f);
        v[a * 4 + r] = x;
        if (key < 40) mx = fmaxf(mx, x);
      }
    mx = red16_max(mx); mx = red32_max(mx);
    float sum = 0.f;
#pragma unroll
    for (int a = 0; a < 3; ++a)
#pragma unroll
      for (int r = 0; r < 4; ++r) {
        int key = a * 16 + g * 4 + r;
        float e = key < 40 ? __expf(v[a * 4 + r] - mx) : 0.f;
        v[a * 4 + r] = e; sum += e;
      }
    sum = red16_add(sum); sum = red32_add(sum);
    float inv = 1.0f / sum;
#pragma unroll
    for (int r = 0; r < 4; ++r) {
      pb[qt][0][r] = (short)f2bf(v[r] * inv); pb[qt][0][4 + r] = (short)f2bf(v[4 + r] * inv);
      pb[qt][1][r] = (short)f2bf(v[8 + r] * inv); pb[qt][1][4 + r] = 0;
    }
  }
  asm volatile("s_waitcnt vmcnt(0)" ::: "memory");
#pragma unroll
  for (int i = 0; i < 4; ++i) { int c = i * 64 + lane; if (c < 200) *(u32x4*)(vL + 40 * 200 + c * 8) = (u32x4){0u, 0u, 0u, 0u}; }
  asm volatile("s_waitcnt lgkmcnt(0)" ::: "memory");
  u32x2 ov[12][2];
#pragma unroll
  for (int et = 0; et < 12; ++et) {
    int r0 = g * 4 + q4;
    bf16x8 a0 = cat8(tr_read(vL + r0 * 200 + et * 16 + p4 * 4), tr_read(vL + (16 + r0) * 200 + et * 16 + p4 * 4));
    bf16x4 t2 = tr_read(vL + (32 + r0) * 200 + et * 16 + p4 * 4);
    bf16x8 a1 = cat8(t2, t2);
#pragma unroll
    for (int qt = 0; qt < 2; ++qt) {
      f32x4 acc = {0.f, 0.f, 0.f, 0.f};
      acc = mfma16(a0, pb[qt][0], acc);
      acc = mfma16(a1, pb[qt][1], acc);
      ov[et][qt] = (u32x2){pack2(acc[0], acc[1]), pack2(acc[2], acc[3])};
    }
  }
  asm volatile("s_waitcnt lgkmcnt(0)" ::: "memory");
#pragma unroll
  for (int et = 0; et < 12; ++et)
#pragma unroll
    for (int qt = 0; qt < 2; ++qt) *(u32x2*)(vL + (qt * 16 + fr) * 200 + et * 16 + g * 4) = ov[et][qt];
  asm volatile("s_waitcnt lgkmcnt(0)" ::: "memory");
#pragma unroll
  for (int i = 0; i < 12; ++i) {
    int c = i * 64 + lane; int row = c / 24, ch = c % 24;
    u32x4 v = *(const u32x4*)(vL + row * 200 + ch * 8);
    st_sc1_16(rW, WS_OX + (u32)((s * SEG_ + row) * HE_ + h * E_ + ch * 8) * 2, v);
  }
}

template <int NS>
__device__ __forceinline__ void redln_row(char* ws, rsrc_t rW, int row) {
  const FP p = getp();
  const int lane = threadIdx.x & 63;
  const bool two = lane < 16;
  float v[16];
#pragma unroll
  for (int hf = 0; hf < 2; ++hf) {
    int c0 = (hf == 0 ? lane : (two ? 64 + lane : lane)) * 8;
    const f32x4* sl = (const f32x4*)((const float*)(ws + WS_SLAB) + (size_t)row * D_ + c0);
    f32x4 lo[NS], hi[NS];
#pragma unroll
    for (int q = 0; q < NS; ++q) { lo[q] = sl[(size_t)q * (S_ * D_ / 4)]; hi[q] = sl[(size_t)q * (S_ * D_ / 4) + 1]; }
    f32x4 s0 = ((lo[0] + lo[1]) + (lo[2] + lo[3])) + ((lo[4] + lo[5]) + (lo[6] + lo[7]));
    f32x4 s1 = ((hi[0] + hi[1]) + (hi[2] + hi[3])) + ((hi[4] + hi[5]) + (hi[6] + hi[7]));
    if (NS == 12) { s0 += (lo[8 % NS] + lo[9 % NS]) + (lo[10 % NS] + lo[11 % NS]); s1 += (hi[8 % NS] + hi[9 % NS]) + (hi[10 % NS] + hi[11 % NS]); }
    u32x4 xw = *(const u32x4*)(p.x + (size_t)row * D_ + c0);
    u32x4 hw;
#pragma unroll
    for (int i = 0; i < 2; ++i) {
      hw[i] = pack2(rbf(s0[2 * i]) + bf2f((u16)(xw[i] & 0xffff)), rbf(s0[2 * i + 1]) + bf2f((u16)(xw[i] >> 16)));
      hw[2 + i] = pack2(rbf(s1[2 * i]) + bf2f((u16)(xw[2 + i] & 0xffff)), rbf(s1[2 * i + 1]) + bf2f((u16)(xw[2 + i] >> 16)));
    }
#pragma unroll
    for (int i = 0; i < 4; ++i) { v[hf * 8 + 2 * i] = bf2f((u16)(hw[i] & 0xffff)); v[hf * 8 + 2 * i + 1] = bf2f((u16)(hw[i] >> 16)); }
    if (hf == 0 || two) st_sc1_16(rW, WS_H + (u32)(row * D_ + c0) * 2, hw);
  }
  float sum = 0.f;
#pragma unroll
  for (int i = 0; i < 8; ++i) sum += v[i] + (two ? v[8 + i] : 0.f);
#pragma unroll
  for (int m = 32; m >= 1; m >>= 1) sum += __shfl_xor(sum, m);
  float mean = sum * (1.0f / D_), sq = 0.f;
#pragma unroll
  for (int i = 0; i < 8; ++i) { float d = v[i] - mean, e = v[8 + i] - mean; sq += d * d + (two ? e * e : 0.f); }
#pragma unroll
  for (int m = 32; m >= 1; m >>= 1) sq += __shfl_xor(sq, m);
  float mbf = rbf(mean), den = rbf(sqrtf(rbf(rbf(sq * (1.0f / D_)) + 1e-5f)));
#pragma unroll
  for (int hf = 0; hf < 2; ++hf) {
    u32x4 o;
#pragma unroll
    for (int i = 0; i < 4; ++i) o[i] = pack2(rbf(v[hf * 8 + 2 * i] - mbf) / den, rbf(v[hf * 8 + 2 * i + 1] - mbf) / den);
    if (hf == 0 || two) st_sc1_16(rW, WS_HN + (u32)(row * D_ + (hf == 0 ? lane : 64 + lane) * 8) * 2, o);
  }
}

template <bool FINE>
__device__ __forceinline__ void phase_b(int mb, int gb, char* ws, rsrc_t rW, char* smem) {
  const int wave = __builtin_amdgcn_readfirstlane(threadIdx.x >> 6);
  if (FINE || gb < 32) {
    const int hh = gb & 31;
    wait_flag(ws, WS_FQKV + (u32)(mb * 96 + 64 + hh) * 64, 1);
#pragma unroll 1
    for (int j = 0; j < 4; ++j) wait_flag(ws, WS_FCH + (u32)(hh * 4 + j) * 64, (u32)(mb + 1));
    acquire_bar();
    if (mb == TLMB) TL((FINE ? 0 : 128) + gb, 16);
    if (wave < (FINE ? 2 : 8)) attn_wave(ws, rW, mb * 8 + (FINE ? (gb >> 5) * 2 : 0) + wave, hh, (u16*)(smem + wave * 19200));
    signal_flag(ws, WS_FAT + (u32)mb * 64);
    if (mb == TLMB) TL((FINE ? 0 : 128) + gb, 17);
  }
  if (FINE) {
    if (gb < 120) {
      int mi = gb & 1, nt = (gb >> 1) % 5, ks = gb / 10;
      wait_flag(ws, WS_FAT + (u32)mb * 64, 128);
      acquire_bar();
    if (mb == TLMB) TL((FINE ? 0 : 128) + gb, 18);
      const FP p = getp();
      gtile<2, 2, 2, E_SLAB>((const u16*)(ws + WS_OX) + ks * 512, HE_, p.Wo + (size_t)ks * 512 * D_, nullptr, D_, 8, mb * 256 + mi * 128, nt * 128, rW,
                             WS_SLAB + (u32)ks * (S_ * D_ * 4), D_, nullptr, nullptr, smem);
      signal_flag(ws, WS_FOP + (u32)mb * 64);
    if (mb == TLMB) TL((FINE ? 0 : 128) + gb, 19);
    }
  } else if (gb >= 32 && gb < 72) {
    int t = gb - 32, nt = t % 5, ks = t / 5;
    wait_flag(ws, WS_FAT + (u32)mb * 64, 32);
    acquire_bar();
    if (mb == TLMB) TL((FINE ? 0 : 128) + gb, 18);
    const FP p = getp();
    gtile<4, 2, 1, E_SLAB>((const u16*)(ws + WS_OX) + ks * 768, HE_, p.Wo + (size_t)ks * 768 * D_, nullptr, D_, 12, mb * 256, nt * 128, rW,
                           WS_SLAB + (u32)ks * (S_ * D_ * 4), D_, nullptr, nullptr, smem);
    signal_flag(ws, WS_FOP + (u32)mb * 64);
    if (mb == TLMB) TL((FINE ? 0 : 128) + gb, 19);
  }
  if (FINE || (gb >= 32 && gb < 54)) {
    wait_flag(ws, WS_FOP + (u32)mb * 64, FINE ? 120 : 40);
    acquire_bar();
    if (mb == TLMB) TL((FINE ? 0 : 128) + gb, 20);
    const int rr = FINE ? gb * 2 + wave : (gb - 32) * 12 + wave;
    if (FINE ? wave < 2 : rr < 256) redln_row<FINE ? 12 : 8>(ws, rW, mb * 256 + rr);
    signal_flag(ws, WS_FLN + (u32)mb * 64);
    if (mb == TLMB) TL((FINE ? 0 : 128) + gb, 21);
  }
  if (FINE) {
    if (gb < 64) {
      wait_flag(ws, WS_FLN + (u32)mb * 64, 128);
      acquire_bar();
    if (mb == TLMB) TL((FINE ? 0 : 128) + gb, 22);
      const FP p = getp();
      gtile<1, 3, 2, E_GLU>((const u16*)(ws + WS_HN), D_, p.wg, p.wu, F_, 10, mb * 256 + (gb & 3) * 64, (gb >> 2) * 96, rW, WS_GU, F_, nullptr, nullptr, smem);
      signal_flag(ws, WS_FGL + (u32)mb * 64);
    if (mb == TLMB) TL((FINE ? 0 : 128) + gb, 23);
    } else if (gb < 104) {
      wait_flag(ws, WS_FGL + (u32)mb * 64, 64);
      acquire_bar();
    if (mb == TLMB) TL((FINE ? 0 : 128) + gb, 24);
      const FP p = getp();
      gtile<1, 1, 4, E_DOWN>((const u16*)(ws + WS_GU), F_, p.wd, nullptr, D_, 24, mb * 256 + ((gb - 64) & 3) * 64, ((gb - 64) >> 2) * 64, rW, 0, D_, (const u16*)(ws + WS_H), p.out, smem);
      __syncthreads();
    if (mb == TLMB) TL((FINE ? 0 : 128) + gb, 25);
    }
  } else {
    if (gb >= 72 && gb < 88) {
      wait_flag(ws, WS_FLN + (u32)mb * 64, 22);
      acquire_bar();
    if (mb == TLMB) TL((FINE ? 0 : 128) + gb, 22);
      const FP p = getp();
      gtile<4, 3, 1, E_GLU>((const u16*)(ws + WS_HN), D_, p.wg, p.wu, F_, 10, mb * 256, (gb - 72) * 96, rW, WS_GU, F_, nullptr, nullptr, smem);
      signal_flag(ws, WS_FGL + (u32)mb * 64);
    if (mb == TLMB) TL((FINE ? 0 : 128) + gb, 23);
    } else if (gb >= 88) {
      wait_flag(ws, WS_FGL + (u32)mb * 64, 16);
      acquire_bar();
    if (mb == TLMB) TL((FINE ? 0 : 128) + gb, 24);
      const FP p = getp();
      gtile<1, 1, 4, E_DOWN>((const u16*)(ws + WS_GU), F_, p.wd, nullptr, D_, 24, mb * 256 + ((gb - 88) & 3) * 64, ((gb - 88) >> 2) * 64, rW, 0, D_, (const u16*)(ws + WS_H), p.out, smem);
      __syncthreads();
    if (mb == TLMB) TL((FINE ? 0 : 128) + gb, 25);
    }
  }
}

__device__ __forceinline__ void gemm_role(int gb, char* smem) {
  char* ws = getp().ws;
  rsrc_t rW = __builtin_amdgcn_make_buffer_rsrc(ws, 0, WS_END, 0x00020000);
  TL(128 + gb, 0);
  {
    int n = gb >> 1, h = n & 31;
    const FP p = getp();
    gtile<2, 3, 2, E_BF16>((const u16*)(ws + WS_XN), D_, n < 32 ? p.Wk : p.Wv, nullptr, HE_, 10, (gb & 1) * 128, h * E_, rW, n < 32 ? WS_KX : WS_VX, HE_, nullptr, nullptr, smem);
    signal_flag(ws, WS_FQKV + (u32)n * 64);
  }
#pragma unroll 1
  for (int a = gb + 64; a < 768; a += 128) {
    int mb = a / 96, n = a % 96; int mat = n >> 5, h = n & 31;
    const FP p = getp();
    const u16* W = mat == 0 ? p.Wk : (mat == 1 ? p.Wv : p.Wq);
    u32 coff = mat == 0 ? WS_KX : (mat == 1 ? WS_VX : WS_QX);
    gtile<4, 3, 1, E_BF16>((const u16*)(ws + WS_XN), D_, W, W, HE_, 10, mb * 256, h * E_, rW, coff, HE_, nullptr, nullptr, smem);
    signal_flag(ws, WS_FQKV + (u32)(mb * 96 + n) * 64);
    TL(128 + gb, 1 + a / 128);
  }
#pragma unroll 1
  for (int mb = 0; mb < 7; ++mb) {
    phase_b<false>(mb, gb, ws, rW, smem);
    TL(128 + gb, 8 + mb);
  }
}

constexpr int MST = 656, QST = 80, VST = 208, PST = 72;
constexpr int CL_MEM = 0;
constexpr int CL_Q   = 8 * MST * 2;
constexpr int KBUF   = 48 * QST * 2;
constexpr int VBUF   = 40 * VST * 2;
constexpr int CL_K   = CL_Q + 8 * QST * 2;
constexpr int CL_V   = CL_K + 2 * KBUF;
constexpr int CL_P   = CL_V + 2 * VBUF;
constexpr int CL_F   = CL_P + 8 * PST * 2;
constexpr int CL_END = CL_F + 16;
constexpr int CL_STG = CL_Q;

#define NPOLL 11
#define STAMP(i) do {} while (0)

__device__ __forceinline__ void chain_role(int cu, char* smem) {
  const FP p = getp();
  const int h = cu >> 2, j = cu & 3;
  const int tid = threadIdx.x, lane = tid & 63;
  const int wave = __builtin_amdgcn_readfirstlane(tid >> 6);
  const int fr = lane & 15, g = lane >> 4, q4 = fr >> 2, p4 = fr & 3;
  char* ws = p.ws;
  u32* ctl = (u32*)(ws + WS_CTL);
  rsrc_t rW = __builtin_amdgcn_make_buffer_rsrc(ws, 0, WS_END, 0x00020000);
  const u32 a2base = WS_GA2 + (u32)(cu % A2_REP) * A2_STRIDE;

  TL(cu, 0);
  bf16x8 Wf[20];
  bf16x8 Wof[6];
  {
#pragma unroll 1
    for (int rnd = 0; rnd < 2; ++rnd) {
      if (rnd) __syncthreads();
#pragma unroll 1
      for (int ii = wave; ii < 120; ii += 12) {
        int m2 = ii / 60, c = (ii % 60) * 64 + lane;
        const u16* sp;
        if (rnd == 1 && m2 == 1) { int e = c / 20, ch = c % 20; sp = p.Wo + (size_t)(h * E_ + e) * D_ + j * 160 + ch * 8; }
        else { const u16* W = rnd == 0 ? (m2 == 0 ? p.Wq : p.Wk) : p.Wv; int d = c / 6, ch = c % 6; sp = W + (size_t)d * HE_ + h * E_ + j * 48 + ch * 8; }
        __builtin_amdgcn_global_load_lds((const u32*)sp, (LAS u32*)(smem + ii * 1024), 16, 0, 0);
      }
      asm volatile("s_waitcnt vmcnt(0)" ::: "memory");
      __syncthreads();
      const int mat = wave / 3;
      if ((rnd == 0 && mat < 2) || (rnd == 1 && mat == 2)) {
        const u16* stg = (const u16*)(smem + (rnd == 0 ? mat : 0) * 61440);
        int e0 = (wave % 3) * 16;
#pragma unroll
        for (int ks = 0; ks < 20; ++ks) {
          int r0 = ks * 32 + g * 8 + q4;
          Wf[ks] = cat8(tr_read(stg + r0 * 48 + e0 + p4 * 4), tr_read(stg + (r0 + 4) * 48 + e0 + p4 * 4));
        }
      }
    }
    const u16* stg = (const u16*)(smem + 61440);
    const int wc = wave < 10 ? wave : 0;
#pragma unroll
    for (int t = 0; t < 6; ++t) {
      int r0 = t * 32 + g * 8 + q4;
      Wof[t] = cat8(tr_read(stg + r0 * 160 + wc * 16 + p4 * 4), tr_read(stg + (r0 + 4) * 160 + wc * 16 + p4 * 4));
    }
    __syncthreads();
  }
  for (int i = tid; i < (CL_END - CL_Q) / 4; i += NTH) ((u32*)(smem + CL_Q))[i] = 0u;
  __syncthreads();

  u16* memL = (u16*)(smem + CL_MEM);
  u16* qL = (u16*)(smem + CL_Q);
  u16* pL = (u16*)(smem + CL_P);
  volatile LAS u32* vflag = (volatile LAS u32*)(smem + CL_F);
  volatile LAS u32* pflag = vflag + 1;
  volatile LAS u32* rflag = vflag + 2;
  bool dead = false;

  auto xfetch = [&](int seg, int bf, int ii, int ln) {
    u32 off;
    if (ii < 5) { int c = ii * 64 + ln; int tok = c / 10, ch = c % 10;
      off = ch < 6 ? WS_KX + (u32)((seg * SEG_ + tok) * HE_ + h * E_ + j * 48 + ch * 8) * 2 : 2048u;
      __builtin_amdgcn_global_load_lds((const u32*)(ws + off), (LAS u32*)(smem + CL_K + bf * KBUF + 8 * QST * 2 + ii * 1024), 16, 0, 16);
    } else { int c = (ii - 5) * 64 + ln; int tok = c / 26, ch = c % 26; if (ch >= 24) ch = 0;
      off = WS_VX + (u32)((seg * SEG_ + tok) * HE_ + h * E_ + ch * 8) * 2;
      __builtin_amdgcn_global_load_lds((const u32*)(ws + off), (LAS u32*)(smem + CL_V + bf * VBUF + 8 * VST * 2 + (ii - 5) * 1024), 16, 0, 16);
    }
  };
  auto wait_kv = [&](int mb) {
    u32* fk = (u32*)(ws + WS_FQKV + (u32)(mb * 96 + h) * 64); u32* fv = (u32*)(ws + WS_FQKV + (u32)(mb * 96 + 32 + h) * 64);
    u32 spins = 0;
    for (;;) {
      u32 a = __hip_atomic_load(fk, RLX_AGENT), b = __hip_atomic_load(fv, RLX_AGENT);
      const u32 need = mb == 0 ? 2u : 1u;
      if (__builtin_amdgcn_readfirstlane(a) >= need && __builtin_amdgcn_readfirstlane(b) >= need) break;
      __builtin_amdgcn_s_sleep(4);
      if (dead || spin_fail(spins, ctl)) { dead = true; break; }
    }
    asm volatile("" ::: "memory");
  };
  TL(cu, 1);
  wait_kv(0);
  TL(cu, 2);
  for (int ii = wave; ii < 18; ii += 12) xfetch(0, 0, ii, lane);
  asm volatile("s_waitcnt vmcnt(0)" ::: "memory");
  __syncthreads();
  u32 vwx[4];
  auto calc_vwx = [&](int bf, int fr_, int g_) {
    const u16* vb = (const u16*)(smem + CL_V + bf * VBUF) + (8 + fr_) * VST + g_ * 8;
    f32x4 a0 = {0.f, 0.f, 0.f, 0.f}, a1 = {0.f, 0.f, 0.f, 0.f};
#pragma unroll
    for (int t = 0; t < 6; ++t) {
      a0 = mfma16(*(const bf16x8*)(vb + t * 32), Wof[t], a0);
      a1 = mfma16(*(const bf16x8*)(vb + 16 * VST + t * 32), Wof[t], a1);
    }
    vwx[0] = pack2(a0[0], a0[1]); vwx[1] = pack2(a0[2], a0[3]); vwx[2] = pack2(a1[0], a1[1]); vwx[3] = pack2(a1[2], a1[3]);
  };
  calc_vwx(0, fr, g);

#pragma unroll 1
  for (int s = 0; s < NSEG_; ++s) {
    int tz = threadIdx.x; asm volatile("" : "+v"(tz));
    const int tid = tz, lane = tz & 63, fr = lane & 15, g = lane >> 4;
    const u32 ep = (u32)s + 1u;
    const u32 par = ep & 1u;
    const int buf = s & 1;
    u16* kL = (u16*)(smem + CL_K + buf * KBUF);
    u16* vL = (u16*)(smem + CL_V + buf * VBUF);
    STAMP(0);
    if (wave < 9 && ((s + 1) & 7) == 0 && s + 1 < NSEG_) wait_kv((s + 1) >> 3);
    if (s == 0) {
      for (int c = tid; c < 8 * 80; c += NTH) { int r = c / 80, ch = c % 80; *(u32x4*)(memL + r * MST + ch * 8) = *(const u32x4*)(getp().mem0 + r * D_ + ch * 8); }
    } else if (wave < 10) {
      const u32 epm = (u32)s;
      u32x4 a0, a1; u32 spins = 0;
      for (;;) {
        asm volatile("" ::: "memory");
        a0 = ld_sc1_16(rW, a2base + tid * 16); a1 = ld_sc1_16(rW, a2base + (tid + 640) * 16);
        bool ok = a0[1] == epm && a0[3] == epm && a1[1] == epm && a1[3] == epm;
        if (__all(ok)) break;
        if (dead || spin_fail(spins, ctl)) { dead = true; break; }
      }
      { int u = tid; int row = ((u >> 4) & 1) * 4 + ((u >> 2) & 3), col = (u >> 5) * 16 + (u & 3) * 4; *(u32x2*)(memL + row * MST + col) = (u32x2){a0[0], a0[2]}; }
      { int u = tid + 640; int row = ((u >> 4) & 1) * 4 + ((u >> 2) & 3), col = (u >> 5) * 16 + (u & 3) * 4; *(u32x2*)(memL + row * MST + col) = (u32x2){a1[0], a1[2]}; }
    }
    STAMP(1);
    lbar();
    STAMP(2);
    if (wave < 9) {
      if (s + 1 < NSEG_) { xfetch(s + 1, buf ^ 1, wave, lane); xfetch(s + 1, buf ^ 1, wave + 9, lane); }
      f32x4 acc = {0.f, 0.f, 0.f, 0.f};
      const u16* mb = memL + (fr & 7) * MST + g * 8;
      bf16x8 b0[4], b1[4];
#pragma unroll
      for (int i = 0; i < 4; ++i) b0[i] = *(const bf16x8*)(mb + i * 32);
#pragma unroll
      for (int gq = 0; gq < 5; ++gq) {
        if (gq & 1) {
          if (gq < 4) {
#pragma unroll
            for (int i = 0; i < 4; ++i) b0[i] = *(const bf16x8*)(mb + ((gq + 1) * 4 + i) * 32);
          }
#pragma unroll
          for (int i = 0; i < 4; ++i) acc = mfma16(Wf[gq * 4 + i], b1[i], acc);
        } else {
          if (gq < 4) {
#pragma unroll
            for (int i = 0; i < 4; ++i) b1[i] = *(const bf16x8*)(mb + ((gq + 1) * 4 + i) * 32);
          }
#pragma unroll
          for (int i = 0; i < 4; ++i) acc = mfma16(Wf[gq * 4 + i], b0[i], acc);
        }
        __builtin_amdgcn_sched_barrier(0);
      }
      u32x2 w = {pack2(acc[0], acc[1]), pack2(acc[2], acc[3])};
      int e = (wave % 3) * 16 + g * 4;
      if (fr < 8) {
        if (wave < 3) *(u32x2*)(qL + fr * QST + e) = w;
        else if (wave < 6) {
          *(u32x2*)(kL + fr * QST + e) = w;
          st_sc1_8(rW, WS_KM + (u32)((s * M_ + fr) * HE_ + h * E_ + j * 48 + e) * 2, w);
        } else {
          st_sc1_16(rW, WS_GB + (u32)(cu * 192 + 96 + (wave - 6) * 32 + fr * 4 + g) * 16, (u32x4){w[0], ep, w[1], ep});
          st_sc1_8(rW, WS_VM + (u32)((s * M_ + fr) * HE_ + h * E_ + j * 48 + e) * 2, w);
        }
      }
    }
    if (s == NSEG_ - 1) break;
    lbar();
    STAMP(3);
    if (wave < 3) {
      f32x4 acc = {0.f, 0.f, 0.f, 0.f};
#pragma unroll
      for (int kk = 0; kk < 2; ++kk) {
        bf16x8 a = *(const bf16x8*)(kL + (wave * 16 + fr) * QST + kk * 32 + g * 8);
        bf16x8 b = *(const bf16x8*)(qL + (fr & 7) * QST + kk * 32 + g * 8);
        acc = mfma16(a, b, acc);
      }
      if (fr < 8)
        st_sc1_16(rW, WS_GB + (u32)(cu * 192 + wave * 32 + fr * 4 + g) * 16,
                  (u32x4){(__float_as_uint(acc[0]) & ~1u) | par, (__float_as_uint(acc[1]) & ~1u) | par,
                          (__float_as_uint(acc[2]) & ~1u) | par, (__float_as_uint(acc[3]) & ~1u) | par});
    }
    STAMP(4);
    if (wave >= NPOLL) {
      const int r = lane & 7, q = lane >> 3;
      bool got = true;
      const u32 o0 = WS_GB + (u32)(h * 4 * 192 + (q >> 2) * 32 + r * 4 + (q & 3)) * 16;
      const u32 o1 = WS_GB + (u32)(h * 4 * 192 + 64 + r * 4 + (q & 1)) * 16;
      u32x4 a[4], b[4]; u32 spins = 0;
      for (;;) {
        asm volatile("" ::: "memory");
        u32 aa = 0xffffffffu, oo = 0u;
#pragma unroll
        for (int i = 0; i < 4; ++i) {
          a[i] = __builtin_amdgcn_raw_buffer_load_b128(rW, o0, i * 3072, 16);
          b[i] = __builtin_amdgcn_raw_buffer_load_b128(rW, o1, i * 3072, 16);
        }
#pragma unroll
        for (int i = 0; i < 4; ++i) {
          aa &= a[i][0] & a[i][1] & a[i][2] & a[i][3] & b[i][0] & b[i][1] & b[i][2] & b[i][3];
          oo |= a[i][0] | a[i][1] | a[i][2] | a[i][3] | b[i][0] | b[i][1] | b[i][2] | b[i][3];
        }
        bool ok = (aa & 1u) == par && (oo & 1u) == par;
        if (__all(ok)) break;
        if (*pflag == ep) { got = false; break; }
        if (dead || spin_fail(spins, ctl)) { dead = true; break; }
      }
      STAMP(5);
      if (got) {
      const float scale = 0.07216878364870322f;
      float sc[8];
#pragma unroll
      for (int k = 0; k < 4; ++k) {
        float x = (__uint_as_float(a[0][k]) + __uint_as_float(a[1][k])) + (__uint_as_float(a[2][k]) + __uint_as_float(a[3][k]));
        float y = (__uint_as_float(b[0][k]) + __uint_as_float(b[1][k])) + (__uint_as_float(b[2][k]) + __uint_as_float(b[3][k]));
        sc[k] = rbf(rbf(x) * scale); sc[4 + k] = rbf(rbf(y) * scale);
      }
      float mx = fmaxf(fmaxf(fmaxf(sc[0], sc[1]), fmaxf(sc[2], sc[3])), fmaxf(fmaxf(sc[4], sc[5]), fmaxf(sc[6], sc[7])));
      mx = fmaxf(mx, __uint_as_float(__builtin_amdgcn_update_dpp(0, __float_as_uint(mx), 0x128, 0xf, 0xf, false)));
      mx = red16_max(mx); mx = red32_max(mx);
      float sum = 0.f, sum2 = 0.f;
#pragma unroll
      for (int k = 0; k < 4; ++k) { sc[k] = __expf(sc[k] - mx); sum += sc[k]; sc[4 + k] = __expf(sc[4 + k] - mx); sum2 += sc[4 + k]; }
      if (q < 2) sum += sum2;
      sum += __uint_as_float(__builtin_amdgcn_update_dpp(0, __float_as_uint(sum), 0x128, 0xf, 0xf, false));
      sum = red16_add(sum); sum = red32_add(sum);
      float inv = __builtin_amdgcn_rcpf(sum);
      *(u32x2*)(pL + r * PST + q * 4) = (u32x2){pack2(sc[0] * inv, sc[1] * inv), pack2(sc[2] * inv, sc[3] * inv)};
      if (q < 2) *(u32x2*)(pL + r * PST + 32 + q * 4) = (u32x2){pack2(sc[4] * inv, sc[5] * inv), pack2(sc[6] * inv, sc[7] * inv)};
      asm volatile("s_waitcnt lgkmcnt(0)" ::: "memory");
      if (lane == 0) *pflag = ep;
      }
      STAMP(6);
    } else if (wave == 9) {
      u32x4 v[6]; u32 spins = 0;
      for (;;) {
        asm volatile("" ::: "memory");
        bool ok = true;
#pragma unroll
        for (int i = 0; i < 6; ++i) { int id = lane + 64 * i; int src = id / 96, u = id % 96; v[i] = ld_sc1_16(rW, WS_GB + (u32)((h * 4 + src) * 192 + 96 + u) * 16); }
#pragma unroll
        for (int i = 0; i < 6; ++i) ok = ok && v[i][1] == ep && v[i][3] == ep;
        if (__all(ok)) break;
        if (dead || spin_fail(spins, ctl)) { dead = true; break; }
      }
#pragma unroll
      for (int i = 0; i < 6; ++i) {
        int id = lane + 64 * i; int src = id / 96, u = id % 96; int vt = u >> 5, rr = (u >> 2) & 7, gg = u & 3;
        *(u32x2*)(vL + rr * VST + src * 48 + vt * 16 + gg * 4) = (u32x2){v[i][0], v[i][2]};
      }
      asm volatile("s_waitcnt lgkmcnt(0)" ::: "memory");
      if (lane == 0) *vflag = ep;
    }
    if (wave < 10) {
      while (*vflag != ep) { if (dead) break; }
      asm volatile("" ::: "memory");
      f32x4 am = {0.f, 0.f, 0.f, 0.f};
      const u16* vb = vL + (fr & 7) * VST + g * 8;
#pragma unroll
      for (int t = 0; t < 6; ++t) am = mfma16(*(const bf16x8*)(vb + t * 32), Wof[t], am);
      u32x4 a0u = {vwx[0], vwx[1], vwx[2], vwx[3]};
      u32x4 a1u = {g < 2 ? pack2(am[0], am[1]) : 0u, g < 2 ? pack2(am[2], am[3]) : 0u, 0u, 0u};
      while (*pflag != ep) { if (dead) break; }
      asm volatile("s_waitcnt vmcnt(0)" ::: "memory");
      const u16* pr = pL + (fr & 7) * PST;
      u32x2 x0 = *(const u32x2*)(pr + 8 + g * 4), x1 = *(const u32x2*)(pr + 24 + g * 4), x2 = *(const u32x2*)(pr + (g & 1) * 4);
      u32x4 b0u = {x0[0], x0[1], x1[0], x1[1]};
      u32x4 b1u = {g < 2 ? x2[0] : 0u, g < 2 ? x2[1] : 0u, 0u, 0u};
      f32x4 acc = {0.f, 0.f, 0.f, 0.f};
      acc = mfma16(__builtin_bit_cast(bf16x8, a0u), __builtin_bit_cast(bf16x8, b0u), acc);
      acc = mfma16(__builtin_bit_cast(bf16x8, a1u), __builtin_bit_cast(bf16x8, b1u), acc);
      if (fr < 8) {
        u32 o = WS_GA1 + (u32)(((j * 10 + wave) * 32 + h) * 32 + fr * 4 + g) * 16;
        st_sc1_16(rW, o, (u32x4){(__float_as_uint(acc[0]) & ~1u) | par, (__float_as_uint(acc[1]) & ~1u) | par,
                                 (__float_as_uint(acc[2]) & ~1u) | par, (__float_as_uint(acc[3]) & ~1u) | par});
      }
    }
    STAMP(8);
    lbar();
    if ((s & 7) == 7) TL(cu, 3 + (s >> 3));
    if ((s & 7) == 7 && tid == 0) __hip_atomic_store((u32*)(ws + WS_FCH + (u32)cu * 64), (u32)(s >> 3) + 1u, RLX_AGENT);
    if (wave < 10) calc_vwx(buf ^ 1, fr, g);
    if (wave >= NPOLL) {
      if (cu < 80) {
        const int ch = lane & 15, hq = lane >> 4;
        bool got = true;
        const u32 rbase = WS_GA1 + (u32)(((cu >> 1) * 32 + hq * 8) * 32 + (cu & 1) * 16 + ch) * 16;
        u32x4 a[8]; u32 spins = 0;
        for (;;) {
          asm volatile("" ::: "memory");
          u32 aa = 0xffffffffu, oo = 0u;
#pragma unroll
          for (int i = 0; i < 8; ++i) a[i] = __builtin_amdgcn_raw_buffer_load_b128(rW, rbase, i * 512, 16);
#pragma unroll
          for (int i = 0; i < 8; ++i) { aa &= a[i][0] & a[i][1] & a[i][2] & a[i][3]; oo |= a[i][0] | a[i][1] | a[i][2] | a[i][3]; }
          bool ok = (aa & 1u) == par && (oo & 1u) == par;
          if (__all(ok)) break;
          if (*rflag == ep) { got = false; break; }
          if (dead || spin_fail(spins, ctl)) { dead = true; break; }
        }
        STAMP(9);
        if (got) {
        float t[4];
#pragma unroll
        for (int k = 0; k < 4; ++k) {
          float x = ((__uint_as_float(a[0][k]) + __uint_as_float(a[1][k])) + (__uint_as_float(a[2][k]) + __uint_as_float(a[3][k])))
                  + ((__uint_as_float(a[4][k]) + __uint_as_float(a[5][k])) + (__uint_as_float(a[6][k]) + __uint_as_float(a[7][k])));
          x = red16_add(x); x = red32_add(x); t[k] = x;
        }
        u32 w0 = pack2(t[0], t[1]), w1 = pack2(t[2], t[3]);
#pragma unroll
        for (int it = 0; it < 4; ++it) st_sc1_16(rW, WS_GA2 + (u32)(cu * 16 + ch) * 16 + (u32)(hq + 4 * it) * A2_STRIDE, (u32x4){w0, ep, w1, ep});
        if (lane == 0) *rflag = ep;
        }
        STAMP(10);
      }
      else if (wave == 11) {
        u32 spins = 0;
        for (;;) {
          asm volatile("" ::: "memory");
          u32x4 a = ld_sc1_16(rW, a2base + (u32)(cu - 80) * 256);
          if (__all(a[1] == ep)) break;
          if (dead || spin_fail(spins, ctl)) { dead = true; break; }
        }
      }
    }
    lbar();
  }
  TL(cu, 10);
  asm volatile("s_waitcnt vmcnt(0)" ::: "memory");
  __syncthreads();
  if (tid == 0) __hip_atomic_store((u32*)(ws + WS_FCH + (u32)cu * 64), 8u, RLX_AGENT);
  phase_b<true>(7, cu, ws, rW, smem);
  TL(cu, 11);
}

__global__ __launch_bounds__(NTH, 3) void k_main(FP p) {
  extern __shared__ __attribute__((aligned(16))) char smem[];
  const int b = blockIdx.x;
  const int id = (b >> 4) * 8 + (b & 7);
  if ((b >> 3) & 1) gemm_role(id, smem);
  else chain_role(id, smem);
}

extern "C" void kernel_launch(void* const* d_in, const int* in_sizes, int n_in,
                              void* d_out, int out_size, void* d_ws, size_t ws_size,
                              hipStream_t stream) {
  char* ws = (char*)d_ws;
  static bool attr_done = false;
  if (!attr_done) {
    (void)hipFuncSetAttribute((const void*)k_main, hipFuncAttributeMaxDynamicSharedMemorySize, LDS_BYTES);
    attr_done = true;
  }
  (void)hipMemsetAsync(ws, 0, WS_ZEND, stream);
  k_ln<<<S_ / 4, 256, 0, stream>>>((const u16*)d_in[0], (u16*)(ws + WS_XN));
  FP p{(const u16*)d_in[0], (const u16*)d_in[1], (const u16*)d_in[2], (const u16*)d_in[3], (const u16*)d_in[4], (const u16*)d_in[5],
       (const u16*)d_in[8], (const u16*)d_in[9], (const u16*)d_in[10], (u16*)d_out, ws};
  k_main<<<256, NTH, LDS_BYTES, stream>>>(p);
}
```

```cpp
#include <hip/hip_runtime.h>
#include <hip/hip_bf16.h>
#include <stdint.h>

typedef unsigned short u16;
typedef unsigned int u32;
typedef unsigned long long u64;
typedef short bf16x8 __attribute__((ext_vector_type(8)));
typedef short bf16x4 __attribute__((ext_vector_type(4)));
typedef float f32x4 __attribute__((ext_vector_type(4)));
typedef u32 u32x4 __attribute__((ext_vector_type(4)));
typedef u32 u32x2 __attribute__((ext_vector_type(2)));
#define LAS __attribute__((address_space(3)))
#define RLX_AGENT __ATOMIC_RELAXED, __HIP_MEMORY_SCOPE_AGENT

constexpr int S_ = 2048, D_ = 640, H_ = 32, E_ = 192, HE_ = H_ * E_;
constexpr int M_ = 8, SEG_ = 32, NSEG_ = 64, F_ = 1536;

constexpr u32 WS_CTL   = 0;
constexpr u32 WS_FQKV  = 4096;
constexpr u32 WS_FCH   = WS_FQKV + 8 * 96 * 64;
constexpr u32 WS_FAT   = WS_FCH + 128 * 64;
constexpr u32 WS_FOP   = WS_FAT + 64 * 64;
constexpr u32 WS_FLN   = WS_FOP + 8 * 64;
constexpr u32 WS_FGL   = WS_FLN + 8 * 64;
constexpr u32 WS_GA2   = (WS_FGL + 8 * 64 + 4095) / 4096 * 4096;
#define A2R 8
constexpr int A2_REP = A2R, A2_STRIDE = 20736;
constexpr u32 WS_GB    = WS_GA2 + A2_REP * A2_STRIDE;
constexpr u32 WS_GA1   = WS_GB + 128 * 3072;
constexpr u32 WS_ZEND  = WS_GA1 + 40 * 32 * 512;
constexpr u32 WS_XN    = (WS_ZEND + 4095) / 4096 * 4096;
constexpr u32 WS_KX    = WS_XN + S_ * D_ * 2;
constexpr u32 WS_VX    = WS_KX + S_ * HE_ * 2;
constexpr u32 WS_QX    = WS_VX + S_ * HE_ * 2;
constexpr u32 WS_KM    = WS_QX + S_ * HE_ * 2;
constexpr u32 WS_VM    = WS_KM + NSEG_ * M_ * HE_ * 2;
constexpr u32 WS_OX    = WS_VM + NSEG_ * M_ * HE_ * 2;
constexpr u32 WS_H     = WS_OX + S_ * HE_ * 2;
constexpr u32 WS_HN    = WS_H + S_ * D_ * 2;
constexpr u32 WS_GU    = WS_HN + S_ * D_ * 2;
constexpr u32 WS_SLAB  = WS_GU + S_ * F_ * 2;
constexpr int NSLAB = 12;
constexpr u32 WS_END   = WS_SLAB + (u32)NSLAB * S_ * D_ * 4;

__device__ __forceinline__ float bf2f(u16 v) { return __uint_as_float(((u32)v) << 16); }
__device__ __forceinline__ u16 f2bf(float f) {
  __hip_bfloat16 b = __float2bfloat16(f);
  return *reinterpret_cast<u16*>(&b);
}
__device__ __forceinline__ float rbf(float f) { return bf2f(f2bf(f)); }
__device__ __forceinline__ u32 pack2(float a, float b) { return (u32)f2bf(a) | ((u32)f2bf(b) << 16); }
__device__ __forceinline__ f32x4 mfma16(bf16x8 a, bf16x8 b, f32x4 c) {
  return __builtin_amdgcn_mfma_f32_16x16x32_bf16(a, b, c, 0, 0, 0);
}
__device__ __forceinline__ bf16x4 tr_read(const void* p) {
  return __builtin_amdgcn_ds_read_tr16_b64_v4i16((LAS bf16x4*)p);
}
__device__ __forceinline__ bf16x8 cat8(bf16x4 a, bf16x4 b) {
  bf16x8 r; r[0]=a[0]; r[1]=a[1]; r[2]=a[2]; r[3]=a[3]; r[4]=b[0]; r[5]=b[1]; r[6]=b[2]; r[7]=b[3]; return r;
}
__device__ __forceinline__ void lbar() { asm volatile("s_waitcnt lgkmcnt(0)\n\ts_barrier" ::: "memory"); }
typedef __amdgpu_buffer_rsrc_t rsrc_t;
__device__ __forceinline__ u32x4 ld_sc1_16(rsrc_t r, u32 off) { return __builtin_amdgcn_raw_buffer_load_b128(r, off, 0, 16); }
__device__ __forceinline__ void st_sc1_16(rsrc_t r, u32 off, u32x4 v) { __builtin_amdgcn_raw_buffer_store_b128(v, r, off, 0, 16); }
__device__ __forceinline__ void st_sc1_8(rsrc_t r, u32 off, u32x2 v) { __builtin_amdgcn_raw_buffer_store_b64(v, r, off, 0, 16); }
__device__ __forceinline__ float red16_max(float x) {
  auto r = __builtin_amdgcn_permlane16_swap(__float_as_uint(x), __float_as_uint(x), false, false);
  return fmaxf(__uint_as_float(r[0]), __uint_as_float(r[1]));
}
__device__ __forceinline__ float red32_max(float x) {
  auto r = __builtin_amdgcn_permlane32_swap(__float_as_uint(x), __float_as_uint(x), false, false);
  return fmaxf(__uint_as_float(r[0]), __uint_as_float(r[1]));
}
__device__ __forceinline__ float red16_add(float x) {
  auto r = __builtin_amdgcn_permlane16_swap(__float_as_uint(x), __float_as_uint(x), false, false);
  return __uint_as_float(r[0]) + __uint_as_float(r[1]);
}
__device__ __forceinline__ float red32_add(float x) {
  auto r = __builtin_amdgcn_permlane32_swap(__float_as_uint(x), __float_as_uint(x), false, false);
  return __uint_as_float(r[0]) + __uint_as_float(r[1]);
}

constexpr u32 SPIN_CAP = 1u << 18;
__device__ __forceinline__ bool spin_fail(u32& spins, u32* ctl) {
  ++spins;
  if (spins > SPIN_CAP) { __hip_atomic_store(ctl, 1u, RLX_AGENT); return true; }
  return (spins & 255u) == 0u && __builtin_amdgcn_readfirstlane(__hip_atomic_load(ctl, RLX_AGENT)) != 0u;
}

struct FP {
  const u16* x; const u16* mem0; const u16* Wq; const u16* Wk; const u16* Wv; const u16* Wo;
  const u16* wg; const u16* wu; const u16* wd; u16* out; char* ws;
};
typedef const __attribute__((address_space(4))) FP* kargp_t;
__device__ __forceinline__ FP getp() {
#if defined(__HIP_DEVICE_COMPILE__)
  kargp_t kp = (kargp_t)__builtin_amdgcn_kernarg_segment_ptr(); asm volatile("" : "+s"(kp)); return *kp;
#else
  return FP{};
#endif
}
constexpr int NTH = 768;
constexpr int LDS_BYTES = 153600;

__device__ __forceinline__ void ln_row(const float (&v)[10], u32 (&o)[5]) {
  float sum = 0.f;
#pragma unroll
  for (int i = 0; i < 10; ++i) sum += v[i];
#pragma unroll
  for (int m = 32; m >= 1; m >>= 1) sum += __shfl_xor(sum, m);
  float mean = sum * (1.0f / D_);
  float sq = 0.f;
#pragma unroll
  for (int i = 0; i < 10; ++i) { float d = v[i] - mean; sq += d * d; }
#pragma unroll
  for (int m = 32; m >= 1; m >>= 1) sq += __shfl_xor(sq, m);
  float var = sq * (1.0f / D_);
  float mb = rbf(mean), vb = rbf(var);
  float den = rbf(sqrtf(rbf(vb + 1e-5f)));
#pragma unroll
  for (int i = 0; i < 5; ++i) o[i] = pack2(rbf(v[2 * i] - mb) / den, rbf(v[2 * i + 1] - mb) / den);
}
__global__ __launch_bounds__(256) void k_ln(const u16* __restrict__ x, u16* __restrict__ y, u32x4* __restrict__ zero) {
  for (u32 i = blockIdx.x * 256 + threadIdx.x; i < WS_ZEND / 16; i += (S_ / 4) * 256) zero[i] = (u32x4){0u, 0u, 0u, 0u};
  int row = blockIdx.x * 4 + (threadIdx.x >> 6);
  int lane = threadIdx.x & 63;
  const u32* xr = (const u32*)(x + (size_t)row * D_);
  float v[10];
#pragma unroll
  for (int i = 0; i < 5; ++i) { u32 w = xr[i * 64 + lane]; v[2 * i] = bf2f((u16)(w & 0xffff)); v[2 * i + 1] = bf2f((u16)(w >> 16)); }
  u32 o[5]; ln_row(v, o);
  u32* yr = (u32*)(y + (size_t)row * D_);
#pragma unroll
  for (int i = 0; i < 5; ++i) yr[i * 64 + lane] = o[i];
}

__device__ __forceinline__ void wait_flag(char* ws, u32 foff, u32 target) {
  if (threadIdx.x == 0) {
    u32* f = (u32*)(ws + foff); u32* ctl = (u32*)(ws + WS_CTL); u32 spins = 0;
    while (__hip_atomic_load(f, RLX_AGENT) < target) {
      __builtin_amdgcn_s_sleep(8);
      if (++spins > (1u << 20)) { __hip_atomic_store(ctl, 1u, RLX_AGENT); break; }
      if ((spins & 63u) == 0u && __hip_atomic_load(ctl, RLX_AGENT) != 0u) break;
    }
  }
}
__device__ __forceinline__ void acquire_bar() {
  if (threadIdx.x == 0) { __builtin_amdgcn_fence(__ATOMIC_ACQUIRE, "agent"); asm volatile("s_waitcnt vmcnt(0)" ::: "memory"); }
  __syncthreads();
}
__device__ __forceinline__ void signal_flag(char* ws, u32 foff) {
  asm volatile("s_waitcnt vmcnt(0)" ::: "memory");
  __syncthreads();
  if (threadIdx.x == 0) __hip_atomic_fetch_add((u32*)(ws + foff), 1u, RLX_AGENT);
}

#define TLMB 3
#define TL(blk, i) do {} while (0)
template <int NWN> __device__ __forceinline__ int bswz(int row) {
  if (NWN == 3) return (((row >> 1) & 1) | (((row >> 3) & 1) << 1)) << 1;
  else if (NWN == 2) return ((row & 3) << 2) | ((row >> 2) & 3);
  else return (((row >> 1) & 1) | (((row >> 3) & 1) << 1)) << 1;
}
enum { E_BF16 = 0, E_SLAB = 1, E_GLU = 2, E_DOWN = 3 };
template <int NWM, int NWN, int KG, int EPI>
__device__ __forceinline__ void gtile(const u16* __restrict__ A, int lda, const u16* __restrict__ B0, const u16* __restrict__ B1, int ldb,
                                      int nk, int m0, int nb, rsrc_t rW, u32 coff, int ldc, const u16* resid, u16* outp, char* smem) {
  constexpr int BM = NWM * 64, BN = NWN * 64, BROW = BN * 2, NA = BM / 16, NI = NA + BN / 16, NT = NI * KG, ST_BYTES = NT * 1024;
  constexpr int NST = (143360 / ST_BYTES) < 16 ? (143360 / ST_BYTES) : 16, PD = NST - 1;
  constexpr int NPW = (NT + 11) / 12;
  constexpr int NW = NWM * NWN;
  int tz = threadIdx.x; asm volatile("" : "+v"(tz));
  const int tid = tz, lane = tid & 63, wave = __builtin_amdgcn_readfirstlane(tid >> 6);
  const int kg = wave / NW, wv = wave % NW, wm = wv % NWM, wn = wv / NWM;
  const int fr = lane & 15, g = lane >> 4;
  const u16* src[NPW]; u32 sstep[NPW];
#pragma unroll
  for (int i = 0; i < NPW; ++i) {
    int it = wave + 12 * i; if (it >= NT) it = 0;
    int sub = it / NI, ii = it % NI;
    if (ii < NA) { int b = ii * 1024 + lane * 16; int row = b >> 6, pos = (b >> 4) & 3, ch = pos ^ ((4 - (row >> 2)) & 3);
      src[i] = A + (size_t)(m0 + row) * lda + ch * 8 + sub * 32; sstep[i] = 32 * KG; }
    else { int b = (ii - NA) * 1024 + lane * 16; int row = b / BROW, pos = (b % BROW) >> 4;
      int ch = pos ^ bswz<NWN>(row);
      if (EPI == E_GLU) { int w2 = ch >> 3, within = ch & 7; const u16* base = (within >> 2) ? B1 : B0;
        src[i] = base + (size_t)(row + sub * 32) * ldb + nb + w2 * 32 + (within & 3) * 8; }
      else src[i] = B0 + (size_t)(row + sub * 32) * ldb + nb + ch * 8;
      sstep[i] = 32u * KG * (u32)ldb; }
  }
  const int npw = (wave + 12 * (NPW - 1)) < NT ? NPW : NPW - 1;
  auto stage = [&](int kt, int buf) {
    char* sb = smem + buf * ST_BYTES;
#pragma unroll
    for (int i = 0; i < NPW; ++i)
      if (i < npw) __builtin_amdgcn_global_load_lds((const u32*)(src[i] + (size_t)kt * sstep[i]), (LAS u32*)(sb + (wave + 12 * i) * 1024), 16, 0, 0);
  };
  f32x4 acc[4][4];
#pragma unroll
  for (int a = 0; a < 4; ++a)
#pragma unroll
    for (int b = 0; b < 4; ++b) acc[a][b] = (f32x4){0.f, 0.f, 0.f, 0.f};
  const bool compk = wave < NW * KG;
  const bool comp = wave < NW;
  const int nst = nk * 2 / KG;
#pragma unroll 1
  for (int i = 0; i < PD; ++i) if (i < nst) stage(i, i);
  int cb = 0, pb = PD;
  const int q = fr >> 2, pp = fr & 3;
  const int r0 = g * 8 + q, r1 = r0 + 4;
  const int x0 = bswz<NWN>(r0), x1 = bswz<NWN>(r1);
  const int aoff = kg * NI * 1024 + (wm * 64 + fr) * 64 + ((g ^ ((4 - (fr >> 2)) & 3)) << 4);
#pragma unroll 1
  for (int kt = 0; kt < nst; ++kt) {
    if (kt + PD <= nst) {
      if (npw == NPW) asm volatile("s_waitcnt vmcnt(%0)" :: "i"(NPW * (PD - 1)) : "memory");
      else asm volatile("s_waitcnt vmcnt(%0)" :: "i"((NPW - 1) * (PD - 1)) : "memory");
    }
    else asm volatile("s_waitcnt vmcnt(0)" ::: "memory");
    lbar();
    if (kt + PD < nst) stage(kt + PD, pb);
    if (compk) {
      const char* sa = smem + cb * ST_BYTES; const char* sb = sa + (kg * NI + NA) * 1024;
      bf16x8 af[4], bfr[4];
#pragma unroll
      for (int a = 0; a < 4; ++a) af[a] = *(const bf16x8*)(sa + aoff + a * 1024);
#pragma unroll
      for (int b = 0; b < 4; ++b) {
        int ch = wn * 8 + b * 2 + (pp >> 1);
        bfr[b] = cat8(tr_read(sb + r0 * BROW + ((ch ^ x0) << 4) + 8 * (pp & 1)), tr_read(sb + r1 * BROW + ((ch ^ x1) << 4) + 8 * (pp & 1)));
      }
#pragma unroll
      for (int a = 0; a < 4; ++a)
#pragma unroll
        for (int b = 0; b < 4; ++b) acc[a][b] = mfma16(bfr[b], af[a], acc[a][b]);
    }
    cb = cb == NST - 1 ? 0 : cb + 1; pb = pb == NST - 1 ? 0 : pb + 1;
  }
  if (KG > 1) {
    lbar();
    if (compk && kg > 0) {
      f32x4* d = (f32x4*)(smem + (wave - NW) * 16384) + lane;
#pragma unroll
      for (int a = 0; a < 4; ++a)
#pragma unroll
        for (int b = 0; b < 4; ++b) d[(a * 4 + b) * 64] = acc[a][b];
    }
    lbar();
    if (comp) {
#pragma unroll
      for (int k2 = 1; k2 < KG; ++k2) {
        const f32x4* d = (const f32x4*)(smem + ((k2 - 1) * NW + wv) * 16384) + lane;
#pragma unroll
        for (int a = 0; a < 4; ++a)
#pragma unroll
          for (int b = 0; b < 4; ++b) acc[a][b] += d[(a * 4 + b) * 64];
      }
    }
  }
  if (EPI == E_DOWN) {
    if (comp) {
#pragma unroll
      for (int a = 0; a < 4; ++a) {
        const int m = m0 + wm * 64 + a * 16 + fr;
#pragma unroll
        for (int b = 0; b < 4; ++b) {
          int n = nb + wn * 64 + b * 16 + g * 4;
          u32x2 rv = *(const u32x2*)(resid + (size_t)m * ldc + n);
          float o0 = rbf(acc[a][b][0]) + bf2f((u16)(rv[0] & 0xffff)), o1 = rbf(acc[a][b][1]) + bf2f((u16)(rv[0] >> 16));
          float o2 = rbf(acc[a][b][2]) + bf2f((u16)(rv[1] & 0xffff)), o3 = rbf(acc[a][b][3]) + bf2f((u16)(rv[1] >> 16));
          *(u32x2*)(outp + (size_t)m * ldc + n) = (u32x2){pack2(o0, o1), pack2(o2, o3)};
        }
      }
    }
  } else {
    constexpr int OC = EPI == E_GLU ? NWN * 32 : BN;
    constexpr int ES = EPI == E_SLAB ? 4 : 2;
    constexpr int RB = OC * ES, RS = RB + 16, CPR = RB / 16;
    lbar();
    if (comp) {
#pragma unroll
      for (int a = 0; a < 4; ++a) {
        char* rowp = smem + (wm * 64 + a * 16 + fr) * RS;
        if (EPI == E_GLU) {
#pragma unroll
          for (int b = 0; b < 2; ++b) {
            float o[4];
#pragma unroll
            for (int r = 0; r < 4; ++r) o[r] = rbf(acc[a][b][r]) * rbf(acc[a][b + 2][r]);
            *(u32x2*)(rowp + (wn * 32 + b * 16 + g * 4) * 2) = (u32x2){pack2(o[0], o[1]), pack2(o[2], o[3])};
          }
        } else {
#pragma unroll
          for (int b = 0; b < 4; ++b) {
            if (EPI == E_BF16) *(u32x2*)(rowp + (wn * 64 + b * 16 + g * 4) * 2) = (u32x2){pack2(acc[a][b][0], acc[a][b][1]), pack2(acc[a][b][2], acc[a][b][3])};
            else *(f32x4*)(rowp + (wn * 64 + b * 16 + g * 4) * 4) = acc[a][b];
          }
        }
      }
    }
    lbar();
#pragma unroll 2
    for (int c = tid; c < BM * CPR; c += NTH) {
      int row = c / CPR, ch = c % CPR;
      u32x4 v = *(const u32x4*)(smem + row * RS + ch * 16);
      st_sc1_16(rW, coff + (u32)((m0 + row) * ldc + nb) * ES + ch * 16, v);
    }
  }
}

__device__ __forceinline__ void attn_wave(const char* ws, rsrc_t rW, int s, int h, u16* vL) {
  int tz = threadIdx.x; asm volatile("" : "+v"(tz));
  const int lane = tz & 63, fr = lane & 15, g = lane >> 4, q4 = fr >> 2, p4 = fr & 3;
  const u16* qx = (const u16*)(ws + WS_QX); const u16* kx = (const u16*)(ws + WS_KX); const u16* vx = (const u16*)(ws + WS_VX);
  const u16* km = (const u16*)(ws + WS_KM); const u16* vm = (const u16*)(ws + WS_VM);
#pragma unroll
  for (int i = 0; i < 16; ++i) {
    int c = i * 64 + lane; int row = c / 25, ch = c % 25; if (row > 39) row = 39; if (ch > 23) ch = 23;
    const u16* sp = row < 8 ? vm + ((size_t)(s * M_ + row)) * HE_ : vx + ((size_t)(s * SEG_ + row - 8)) * HE_;
    __builtin_amdgcn_global_load_lds((const u32*)(sp + h * E_ + ch * 8), (LAS u32*)((char*)vL + i * 1024), 16, 0, 0);
  }
  f32x4 sc[3][2];
#pragma unroll
  for (int a = 0; a < 3; ++a) { sc[a][0] = (f32x4){0.f, 0.f, 0.f, 0.f}; sc[a][1] = (f32x4){0.f, 0.f, 0.f, 0.f}; }
  {
    const u16* kp[3];
#pragma unroll
    for (int a = 0; a < 3; ++a) {
      int key = a * 16 + fr; if (key > 39) key = 39;
      kp[a] = (key < 8 ? km + ((size_t)(s * M_ + key)) * HE_ : kx + ((size_t)(s * SEG_ + key - 8)) * HE_) + h * E_ + g * 8;
    }
    const u16* qp0 = qx + ((size_t)(s * SEG_ + fr)) * HE_ + h * E_ + g * 8;
    const u16* qp1 = qp0 + (size_t)16 * HE_;
#pragma unroll
    for (int hf = 0; hf < 2; ++hf) {
      bf16x8 qf[3][2], kf[3][3];
#pragma unroll
      for (int kk = 0; kk < 3; ++kk) {
        qf[kk][0] = *(const bf16x8*)(qp0 + (hf * 3 + kk) * 32); qf[kk][1] = *(const bf16x8*)(qp1 + (hf * 3 + kk) * 32);
#pragma unroll
        for (int a = 0; a < 3; ++a) kf[kk][a] = *(const bf16x8*)(kp[a] + (hf * 3 + kk) * 32);
      }
#pragma unroll
      for (int kk = 0; kk < 3; ++kk)
#pragma unroll
        for (int a = 0; a < 3; ++a) { sc[a][0] = mfma16(kf[kk][a], qf[kk][0], sc[a][0]); sc[a][1] = mfma16(kf[kk][a], qf[kk][1], sc[a][1]); }
    }
  }
  const float scale = 0.07216878364870322f;
  bf16x8 pb[2][2];
#pragma unroll
  for (int qt = 0; qt < 2; ++qt) {
    int qrow = qt * 16 + fr;
    float v[12]; float mx = -3.0e38f;
#pragma unroll
    for (int a = 0; a < 3; ++a)
#pragma unroll
      for (int r = 0; r < 4; ++r) {
        int key = a * 16 + g * 4 + r;
        float x = rbf(rbf(sc[a][qt][r]) * scale);
        bool vis = key < 8 || (key - 8) <= qrow;
        x = vis ? x : rbf(-1000000000.0f);
        v[a * 4 + r] = x;
        if (key < 40) mx = fmaxf(mx, x);
      }
    mx = red16_max(mx); mx = red32_max(mx);
    float sum = 0.f;
#pragma unroll
    for (int a = 0; a < 3; ++a)
#pragma unroll
      for (int r = 0; r < 4; ++r) {
        int key = a * 16 + g * 4 + r;
        float e = key < 40 ? __expf(v[a * 4 + r] - mx) : 0.f;
        v[a * 4 + r] = e; sum += e;
      }
    sum = red16_add(sum); sum = red32_add(sum);
    float inv = 1.0f / sum;
#pragma unroll
    for (int r = 0; r < 4; ++r) {
      pb[qt][0][r] = (short)f2bf(v[r] * inv); pb[qt][0][4 + r] = (short)f2bf(v[4 + r] * inv);
      pb[qt][1][r] = (short)f2bf(v[8 + r] * inv); pb[qt][1][4 + r] = 0;
    }
  }
  asm volatile("s_waitcnt vmcnt(0)" ::: "memory");
#pragma unroll
  for (int i = 0; i < 4; ++i) { int c = i * 64 + lane; if (c < 200) *(u32x4*)(vL + 40 * 200 + c * 8) = (u32x4){0u, 0u, 0u, 0u}; }
  asm volatile("s_waitcnt lgkmcnt(0)" ::: "memory");
  u32x2 ov[12][2];
#pragma unroll
  for (int et = 0; et < 12; ++et) {
    int r0 = g * 4 + q4;
    bf16x8 a0 = cat8(tr_read(vL + r0 * 200 + et * 16 + p4 * 4), tr_read(vL + (16 + r0) * 200 + et * 16 + p4 * 4));
    bf16x4 t2 = tr_read(vL + (32 + r0) * 200 + et * 16 + p4 * 4);
    bf16x8 a1 = cat8(t2, t2);
#pragma unroll
    for (int qt = 0; qt < 2; ++qt) {
      f32x4 acc = {0.f, 0.f, 0.f, 0.f};
      acc = mfma16(a0, pb[qt][0], acc);
      acc = mfma16(a1, pb[qt][1], acc);
      ov[et][qt] = (u32x2){pack2(acc[0], acc[1]), pack2(acc[2], acc[3])};
    }
  }
  asm volatile("s_waitcnt lgkmcnt(0)" ::: "memory");
#pragma unroll
  for (int et = 0; et < 12; ++et)
#pragma unroll
    for (int qt = 0; qt < 2; ++qt) *(u32x2*)(vL + (qt * 16 + fr) * 200 + et * 16 + g * 4) = ov[et][qt];
  asm volatile("s_waitcnt lgkmcnt(0)" ::: "memory");
#pragma unroll
  for (int i = 0; i < 12; ++i) {
    int c = i * 64 + lane; int row = c / 24, ch = c % 24;
    u32x4 v = *(const u32x4*)(vL + row * 200 + ch * 8);
    st_sc1_16(rW, WS_OX + (u32)((s * SEG_ + row) * HE_ + h * E_ + ch * 8) * 2, v);
  }
}

template <int NS>
__device__ __forceinline__ void redln_row(char* ws, rsrc_t rW, int row) {
  const FP p = getp();
  const int lane = threadIdx.x & 63;
  const bool two = lane < 16;
  float v[16];
#pragma unroll
  for (int hf = 0; hf < 2; ++hf) {
    int c0 = (hf == 0 ? lane : (two ? 64 + lane : lane)) * 8;
    const f32x4* sl = (const f32x4*)((const float*)(ws + WS_SLAB) + (size_t)row * D_ + c0);
    f32x4 lo[NS], hi[NS];
#pragma unroll
    for (int q = 0; q < NS; ++q) { lo[q] = sl[(size_t)q * (S_ * D_ / 4)]; hi[q] = sl[(size_t)q * (S_ * D_ / 4) + 1]; }
    f32x4 s0 = ((lo[0] + lo[1]) + (lo[2] + lo[3])) + ((lo[4] + lo[5]) + (lo[6] + lo[7]));
    f32x4 s1 = ((hi[0] + hi[1]) + (hi[2] + hi[3])) + ((hi[4] + hi[5]) + (hi[6] + hi[7]));
    if (NS == 12) { s0 += (lo[8 % NS] + lo[9 % NS]) + (lo[10 % NS] + lo[11 % NS]); s1 += (hi[8 % NS] + hi[9 % NS]) + (hi[10 % NS] + hi[11 % NS]); }
    u32x4 xw = *(const u32x4*)(p.x + (size_t)row * D_ + c0);
    u32x4 hw;
#pragma unroll
    for (int i = 0; i < 2; ++i) {
      hw[i] = pack2(rbf(s0[2 * i]) + bf2f((u16)(xw[i] & 0xffff)), rbf(s0[2 * i + 1]) + bf2f((u16)(xw[i] >> 16)));
      hw[2 + i] = pack2(rbf(s1[2 * i]) + bf2f((u16)(xw[2 + i] & 0xffff)), rbf(s1[2 * i + 1]) + bf2f((u16)(xw[2 + i] >> 16)));
    }
#pragma unroll
    for (int i = 0; i < 4; ++i) { v[hf * 8 + 2 * i] = bf2f((u16)(hw[i] & 0xffff)); v[hf * 8 + 2 * i + 1] = bf2f((u16)(hw[i] >> 16)); }
    if (hf == 0 || two) st_sc1_16(rW, WS_H + (u32)(row * D_ + c0) * 2, hw);
  }
  float sum = 0.f;
#pragma unroll
  for (int i = 0; i < 8; ++i) sum += v[i] + (two ? v[8 + i] : 0.f);
#pragma unroll
  for (int m = 32; m >= 1; m >>= 1) sum += __shfl_xor(sum, m);
  float mean = sum * (1.0f / D_), sq = 0.f;
#pragma unroll
  for (int i = 0; i < 8; ++i) { float d = v[i] - mean, e = v[8 + i] - mean; sq += d * d + (two ? e * e : 0.f); }
#pragma unroll
  for (int m = 32; m >= 1; m >>= 1) sq += __shfl_xor(sq, m);
  float mbf = rbf(mean), den = rbf(sqrtf(rbf(rbf(sq * (1.0f / D_)) + 1e-5f)));
#pragma unroll
  for (int hf = 0; hf < 2; ++hf) {
    u32x4 o;
#pragma unroll
    for (int i = 0; i < 4; ++i) o[i] = pack2(rbf(v[hf * 8 + 2 * i] - mbf) / den, rbf(v[hf * 8 + 2 * i + 1] - mbf) / den);
    if (hf == 0 || two) st_sc1_16(rW, WS_HN + (u32)(row * D_ + (hf == 0 ? lane : 64 + lane) * 8) * 2, o);
  }
}

template <bool FINE>
__device__ __forceinline__ void phase_b(int mb, int gb, char* ws, rsrc_t rW, char* smem) {
  const int wave = __builtin_amdgcn_readfirstlane(threadIdx.x >> 6);
  if (FINE || gb < 32) {
    const int hh = gb & 31;
    wait_flag(ws, WS_FQKV + (u32)(mb * 96 + 64 + hh) * 64, 1);
#pragma unroll 1
    for (int j = 0; j < 4; ++j) wait_flag(ws, WS_FCH + (u32)(hh * 4 + j) * 64, (u32)(mb + 1));
    acquire_bar();
    if (mb == TLMB) TL((FINE ? 0 : 128) + gb, 16);
    if (wave < (FINE ? 2 : 8)) attn_wave(ws, rW, mb * 8 + (FINE ? (gb >> 5) * 2 : 0) + wave, hh, (u16*)(smem + wave * 19200));
    signal_flag(ws, WS_FAT + (u32)mb * 64);
    if (mb == TLMB) TL((FINE ? 0 : 128) + gb, 17);
  }
  if (FINE) {
    if (gb < 120) {
      int mi = gb & 1, nt = (gb >> 1) % 5, ks = gb / 10;
      wait_flag(ws, WS_FAT + (u32)mb * 64, 128);
      acquire_bar();
    if (mb == TLMB) TL((FINE ? 0 : 128) + gb, 18);
      const FP p = getp();
      gtile<2, 2, 2, E_SLAB>((const u16*)(ws + WS_OX) + ks * 512, HE_, p.Wo + (size_t)ks * 512 * D_, nullptr, D_, 8, mb * 256 + mi * 128, nt * 128, rW,
                             WS_SLAB + (u32)ks * (S_ * D_ * 4), D_, nullptr, nullptr, smem);
      signal_flag(ws, WS_FOP + (u32)mb * 64);
    if (mb == TLMB) TL((FINE ? 0 : 128) + gb, 19);
    }
  } else if (gb >= 32 && gb < 72) {
    int t = gb - 32, nt = t % 5, ks = t / 5;
    wait_flag(ws, WS_FAT + (u32)mb * 64, 32);
    acquire_bar();
    if (mb == TLMB) TL((FINE ? 0 : 128) + gb, 18);
    const FP p = getp();
    gtile<4, 2, 1, E_SLAB>((const u16*)(ws + WS_OX) + ks * 768, HE_, p.Wo + (size_t)ks * 768 * D_, nullptr, D_, 12, mb * 256, nt * 128, rW,
                           WS_SLAB + (u32)ks * (S_ * D_ * 4), D_, nullptr, nullptr, smem);
    signal_flag(ws, WS_FOP + (u32)mb * 64);
    if (mb == TLMB) TL((FINE ? 0 : 128) + gb, 19);
  }
  if (FINE || (gb >= 32 && gb < 54)) {
    wait_flag(ws, WS_FOP + (u32)mb * 64, FINE ? 120 : 40);
    acquire_bar();
    if (mb == TLMB) TL((FINE ? 0 : 128) + gb, 20);
    const int rr = FINE ? gb * 2 + wave : (gb - 32) * 12 + wave;
    if (FINE ? wave < 2 : rr < 256) redln_row<FINE ? 12 : 8>(ws, rW, mb * 256 + rr);
    signal_flag(ws, WS_FLN + (u32)mb * 64);
    if (mb == TLMB) TL((FINE ? 0 : 128) + gb, 21);
  }
  if (FINE) {
    if (gb < 64) {
      wait_flag(ws, WS_FLN + (u32)mb * 64, 128);
      acquire_bar();
    if (mb == TLMB) TL((FINE ? 0 : 128) + gb, 22);
      const FP p = getp();
      gtile<1, 3, 2, E_GLU>((const u16*)(ws + WS_HN), D_, p.wg, p.wu, F_, 10, mb * 256 + (gb & 3) * 64, (gb >> 2) * 96, rW, WS_GU, F_, nullptr, nullptr, smem);
      signal_flag(ws, WS_FGL + (u32)mb * 64);
    if (mb == TLMB) TL((FINE ? 0 : 128) + gb, 23);
    } else if (gb < 104) {
      wait_flag(ws, WS_FGL + (u32)mb * 64, 64);
      acquire_bar();
    if (mb == TLMB) TL((FINE ? 0 : 128) + gb, 24);
      const FP p = getp();
      gtile<1, 1, 4, E_DOWN>((const u16*)(ws + WS_GU), F_, p.wd, nullptr, D_, 24, mb * 256 + ((gb - 64) & 3) * 64, ((gb - 64) >> 2) * 64, rW, 0, D_, (const u16*)(ws + WS_H), p.out, smem);
      __syncthreads();
    if (mb == TLMB) TL((FINE ? 0 : 128) + gb, 25);
    }
  } else {
    if (gb >= 72 && gb < 88) {
      wait_flag(ws, WS_FLN + (u32)mb * 64, 22);
      acquire_bar();
    if (mb == TLMB) TL((FINE ? 0 : 128) + gb, 22);
      const FP p = getp();
      gtile<4, 3, 1, E_GLU>((const u16*)(ws + WS_HN), D_, p.wg, p.wu, F_, 10, mb * 256, (gb - 72) * 96, rW, WS_GU, F_, nullptr, nullptr, smem);
      signal_flag(ws, WS_FGL + (u32)mb * 64);
    if (mb == TLMB) TL((FINE ? 0 : 128) + gb, 23);
    } else if (gb >= 88) {
      wait_flag(ws, WS_FGL + (u32)mb * 64, 16);
      acquire_bar();
    if (mb == TLMB) TL((FINE ? 0 : 128) + gb, 24);
      const FP p = getp();
      gtile<1, 1, 4, E_DOWN>((const u16*)(ws + WS_GU), F_, p.wd, nullptr, D_, 24, mb * 256 + ((gb - 88) & 3) * 64, ((gb - 88) >> 2) * 64, rW, 0, D_, (const u16*)(ws + WS_H), p.out, smem);
      __syncthreads();
    if (mb == TLMB) TL((FINE ? 0 : 128) + gb, 25);
    }
  }
}

__device__ __forceinline__ void gemm_role(int gb, char* smem) {
  char* ws = getp().ws;
  rsrc_t rW = __builtin_amdgcn_make_buffer_rsrc(ws, 0, WS_END, 0x00020000);
  TL(128 + gb, 0);
  {
    int n = gb >> 1, h = n & 31;
    const FP p = getp();
    gtile<2, 3, 2, E_BF16>((const u16*)(ws + WS_XN), D_, n < 32 ? p.Wk : p.Wv, nullptr, HE_, 10, (gb & 1) * 128, h * E_, rW, n < 32 ? WS_KX : WS_VX, HE_, nullptr, nullptr, smem);
    signal_flag(ws, WS_FQKV + (u32)n * 64);
  }
#pragma unroll 1
  for (int a = gb + 64; a < 768; a += 128) {
    int mb = a / 96, n = a % 96; int mat = n >> 5, h = n & 31;
    const FP p = getp();
    const u16* W = mat == 0 ? p.Wk : (mat == 1 ? p.Wv : p.Wq);
    u32 coff = mat == 0 ? WS_KX : (mat == 1 ? WS_VX : WS_QX);
    gtile<4, 3, 1, E_BF16>((const u16*)(ws + WS_XN), D_, W, W, HE_, 10, mb * 256, h * E_, rW, coff, HE_, nullptr, nullptr, smem);
    signal_flag(ws, WS_FQKV + (u32)(mb * 96 + n) * 64);
    TL(128 + gb, 1 + a / 128);
  }
#pragma unroll 1
  for (int mb = 0; mb < 7; ++mb) {
    phase_b<false>(mb, gb, ws, rW, smem);
    TL(128 + gb, 8 + mb);
  }
}

constexpr int MST = 656, QST = 80, VST = 208, PST = 72;
constexpr int CL_MEM = 0;
constexpr int CL_Q   = 8 * MST * 2;
constexpr int KBUF   = 48 * QST * 2;
constexpr int VBUF   = 40 * VST * 2;
constexpr int CL_K   = CL_Q + 8 * QST * 2;
constexpr int CL_V   = CL_K + 2 * KBUF;
constexpr int CL_P   = CL_V + 2 * VBUF;
constexpr int CL_F   = CL_P + 8 * PST * 2;
constexpr int CL_END = CL_F + 16;
constexpr int CL_STG = CL_Q;

#define DLY_A2I 0
#define DLY_S 8
#define DLY_B 5
#define DLY_V 4
#define DLY_A1 0
#define DLY_A2 0
#define NPOLL 11
#define STAMP(i) do {} while (0)

__device__ __forceinline__ void chain_role(int cu, char* smem) {
  const FP p = getp();
  const int h = cu >> 2, j = cu & 3;
  const int tid = threadIdx.x, lane = tid & 63;
  const int wave = __builtin_amdgcn_readfirstlane(tid >> 6);
  const int fr = lane & 15, g = lane >> 4, q4 = fr >> 2, p4 = fr & 3;
  char* ws = p.ws;
  u32* ctl = (u32*)(ws + WS_CTL);
  rsrc_t rW = __builtin_amdgcn_make_buffer_rsrc(ws, 0, WS_END, 0x00020000);
  const u32 a2base = WS_GA2 + (u32)(cu % A2_REP) * A2_STRIDE;

  TL(cu, 0);
  bf16x8 Wf[20];
  bf16x8 Wof[6];
  {
#pragma unroll 1
    for (int rnd = 0; rnd < 2; ++rnd) {
      if (rnd) __syncthreads();
#pragma unroll 1
      for (int ii = wave; ii < 120; ii += 12) {
        int m2 = ii / 60, c = (ii % 60) * 64 + lane;
        const u16* sp;
        if (rnd == 1 && m2 == 1) { int e = c / 20, ch = c % 20; sp = p.Wo + (size_t)(h * E_ + e) * D_ + j * 160 + ch * 8; }
        else { const u16* W = rnd == 0 ? (m2 == 0 ? p.Wq : p.Wk) : p.Wv; int d = c / 6, ch = c % 6; sp = W + (size_t)d * HE_ + h * E_ + j * 48 + ch * 8; }
        __builtin_amdgcn_global_load_lds((const u32*)sp, (LAS u32*)(smem + ii * 1024), 16, 0, 0);
      }
      asm volatile("s_waitcnt vmcnt(0)" ::: "memory");
      __syncthreads();
      const int mat = wave / 3;
      if ((rnd == 0 && mat < 2) || (rnd == 1 && mat == 2)) {
        const u16* stg = (const u16*)(smem + (rnd == 0 ? mat : 0) * 61440);
        int e0 = (wave % 3) * 16;
#pragma unroll
        for (int ks = 0; ks < 20; ++ks) {
          int r0 = ks * 32 + g * 8 + q4;
          Wf[ks] = cat8(tr_read(stg + r0 * 48 + e0 + p4 * 4), tr_read(stg + (r0 + 4) * 48 + e0 + p4 * 4));
        }
      }
    }
    const u16* stg = (const u16*)(smem + 61440);
    const int wc = wave < 10 ? wave : 0;
#pragma unroll
    for (int t = 0; t < 6; ++t) {
      int r0 = t * 32 + g * 8 + q4;
      Wof[t] = cat8(tr_read(stg + r0 * 160 + wc * 16 + p4 * 4), tr_read(stg + (r0 + 4) * 160 + wc * 16 + p4 * 4));
    }
    __syncthreads();
  }
  for (int i = tid; i < (CL_END - CL_Q) / 4; i += NTH) ((u32*)(smem + CL_Q))[i] = 0u;
  __syncthreads();

  u16* memL = (u16*)(smem + CL_MEM);
  u16* qL = (u16*)(smem + CL_Q);
  u16* pL = (u16*)(smem + CL_P);
  volatile LAS u32* vflag = (volatile LAS u32*)(smem + CL_F);
  volatile LAS u32* pflag = vflag + 1;
  volatile LAS u32* rflag = vflag + 2;
  bool dead = false;

  auto xfetch = [&](int seg, int bf, int ii, int ln) {
    u32 off;
    if (ii < 5) { int c = ii * 64 + ln; int tok = c / 10, ch = c % 10;
      off = ch < 6 ? WS_KX + (u32)((seg * SEG_ + tok) * HE_ + h * E_ + j * 48 + ch * 8) * 2 : 2048u;
      __builtin_amdgcn_global_load_lds((const u32*)(ws + off), (LAS u32*)(smem + CL_K + bf * KBUF + 8 * QST * 2 + ii * 1024), 16, 0, 16);
    } else { int c = (ii - 5) * 64 + ln; int tok = c / 26, ch = c % 26; if (ch >= 24) ch = 0;
      off = WS_VX + (u32)((seg * SEG_ + tok) * HE_ + h * E_ + ch * 8) * 2;
      __builtin_amdgcn_global_load_lds((const u32*)(ws + off), (LAS u32*)(smem + CL_V + bf * VBUF + 8 * VST * 2 + (ii - 5) * 1024), 16, 0, 16);
    }
  };
  auto wait_kv = [&](int mb) {
    u32* fk = (u32*)(ws + WS_FQKV + (u32)(mb * 96 + h) * 64); u32* fv = (u32*)(ws + WS_FQKV + (u32)(mb * 96 + 32 + h) * 64);
    u32 spins = 0;
    for (;;) {
      u32 a = __hip_atomic_load(fk, RLX_AGENT), b = __hip_atomic_load(fv, RLX_AGENT);
      const u32 need = mb == 0 ? 2u : 1u;
      if (__builtin_amdgcn_readfirstlane(a) >= need && __builtin_amdgcn_readfirstlane(b) >= need) break;
      __builtin_amdgcn_s_sleep(4);
      if (dead || spin_fail(spins, ctl)) { dead = true; break; }
    }
    asm volatile("" ::: "memory");
  };
  TL(cu, 1);
  wait_kv(0);
  TL(cu, 2);
  for (int ii = wave; ii < 18; ii += 12) xfetch(0, 0, ii, lane);
  asm volatile("s_waitcnt vmcnt(0)" ::: "memory");
  __syncthreads();
  u32 vwx[4];
  auto calc_vwx = [&](int bf, int fr_, int g_) {
    const u16* vb = (const u16*)(smem + CL_V + bf * VBUF) + (8 + fr_) * VST + g_ * 8;
    f32x4 a0 = {0.f, 0.f, 0.f, 0.f}, a1 = {0.f, 0.f, 0.f, 0.f};
#pragma unroll
    for (int t = 0; t < 6; ++t) {
      a0 = mfma16(*(const bf16x8*)(vb + t * 32), Wof[t], a0);
      a1 = mfma16(*(const bf16x8*)(vb + 16 * VST + t * 32), Wof[t], a1);
    }
    vwx[0] = pack2(a0[0], a0[1]); vwx[1] = pack2(a0[2], a0[3]); vwx[2] = pack2(a1[0], a1[1]); vwx[3] = pack2(a1[2], a1[3]);
  };
  calc_vwx(0, fr, g);

#pragma unroll 1
  for (int s = 0; s < NSEG_; ++s) {
    int tz = threadIdx.x; asm volatile("" : "+v"(tz));
    const int tid = tz, lane = tz & 63, fr = lane & 15, g = lane >> 4;
    const u32 ep = (u32)s + 1u;
    const u32 par = ep & 1u;
    const int buf = s & 1;
    u16* kL = (u16*)(smem + CL_K + buf * KBUF);
    u16* vL = (u16*)(smem + CL_V + buf * VBUF);
    STAMP(0);
    if (s == 0) {
      for (int c = tid; c < 8 * 80; c += NTH) { int r = c / 80, ch = c % 80; *(u32x4*)(memL + r * MST + ch * 8) = *(const u32x4*)(getp().mem0 + r * D_ + ch * 8); }
    } else if (wave < 10) {
      const u32 epm = (u32)s;
      u32x4 a0, a1; u32 spins = 0;
      for (int i = 0; i < DLY_A2I; ++i) __builtin_amdgcn_s_sleep(4);
      for (;;) {
        asm volatile("" ::: "memory");
        a0 = ld_sc1_16(rW, a2base + tid * 16); a1 = ld_sc1_16(rW, a2base + (tid + 640) * 16);
        bool ok = a0[1] == epm && a0[3] == epm && a1[1] == epm && a1[3] == epm;
        if (__all(ok)) break;
        for (int i = 0; i < DLY_A2; ++i) __builtin_amdgcn_s_sleep(2);
        if (dead || spin_fail(spins, ctl)) { dead = true; break; }
      }
      { int u = tid; int row = ((u >> 4) & 1) * 4 + ((u >> 2) & 3), col = (u >> 5) * 16 + (u & 3) * 4; *(u32x2*)(memL + row * MST + col) = (u32x2){a0[0], a0[2]}; }
      { int u = tid + 640; int row = ((u >> 4) & 1) * 4 + ((u >> 2) & 3), col = (u >> 5) * 16 + (u & 3) * 4; *(u32x2*)(memL + row * MST + col) = (u32x2){a1[0], a1[2]}; }
    }
    STAMP(1);
    lbar();
    STAMP(2);
    if (wave < 9) {
      f32x4 acc = {0.f, 0.f, 0.f, 0.f};
      const u16* mb = memL + (fr & 7) * MST + g * 8;
      bf16x8 b0[4], b1[4];
#pragma unroll
      for (int i = 0; i < 4; ++i) b0[i] = *(const bf16x8*)(mb + i * 32);
#pragma unroll
      for (int gq = 0; gq < 5; ++gq) {
        if (gq & 1) {
          if (gq < 4) {
#pragma unroll
            for (int i = 0; i < 4; ++i) b0[i] = *(const bf16x8*)(mb + ((gq + 1) * 4 + i) * 32);
          }
#pragma unroll
          for (int i = 0; i < 4; ++i) acc = mfma16(Wf[gq * 4 + i], b1[i], acc);
        } else {
          if (gq < 4) {
#pragma unroll
            for (int i = 0; i < 4; ++i) b1[i] = *(const bf16x8*)(mb + ((gq + 1) * 4 + i) * 32);
          }
#pragma unroll
          for (int i = 0; i < 4; ++i) acc = mfma16(Wf[gq * 4 + i], b0[i], acc);
        }
        __builtin_amdgcn_sched_barrier(0);
      }
      u32x2 w = {pack2(acc[0], acc[1]), pack2(acc[2], acc[3])};
      int e = (wave % 3) * 16 + g * 4;
      if (fr < 8) {
        if (wave < 3) *(u32x2*)(qL + fr * QST + e) = w;
        else if (wave < 6) {
          *(u32x2*)(kL + fr * QST + e) = w;
          st_sc1_8(rW, WS_KM + (u32)((s * M_ + fr) * HE_ + h * E_ + j * 48 + e) * 2, w);
        } else {
          st_sc1_16(rW, WS_GB + (u32)(cu * 192 + 96 + (wave - 6) * 32 + fr * 4 + g) * 16, (u32x4){w[0], ep, w[1], ep});
          st_sc1_8(rW, WS_VM + (u32)((s * M_ + fr) * HE_ + h * E_ + j * 48 + e) * 2, w);
        }
      }
    }
    if (s == NSEG_ - 1) break;
    lbar();
    STAMP(3);
    if (wave == 10) {
      if (((s + 1) & 7) == 0) wait_kv((s + 1) >> 3);
#pragma unroll 1
      for (int ii = 0; ii < 18; ++ii) xfetch(s + 1, buf ^ 1, ii, lane);
    }

    if (wave < 3) {
      f32x4 acc = {0.f, 0.f, 0.f, 0.f};
#pragma unroll
      for (int kk = 0; kk < 2; ++kk) {
        bf16x8 a = *(const bf16x8*)(kL + (wave * 16 + fr) * QST + kk * 32 + g * 8);
        bf16x8 b = *(const bf16x8*)(qL + (fr & 7) * QST + kk * 32 + g * 8);
        acc = mfma16(a, b, acc);
      }
      if (fr < 8)
        st_sc1_16(rW, WS_GB + (u32)(cu * 192 + wave * 32 + fr * 4 + g) * 16,
                  (u32x4){(__float_as_uint(acc[0]) & ~1u) | par, (__float_as_uint(acc[1]) & ~1u) | par,
                          (__float_as_uint(acc[2]) & ~1u) | par, (__float_as_uint(acc[3]) & ~1u) | par});
    }
    STAMP(4);
    if (wave >= NPOLL) {
      const int r = lane & 7, q = lane >> 3;
      bool got = true;
      for (int i = 0; i < DLY_B; ++i) __builtin_amdgcn_s_sleep(4);
      const u32 o0 = WS_GB + (u32)(h * 4 * 192 + (q >> 2) * 32 + r * 4 + (q & 3)) * 16;
      const u32 o1 = WS_GB + (u32)(h * 4 * 192 + 64 + r * 4 + (q & 1)) * 16;
      u32x4 a[4], b[4]; u32 spins = 0;
      for (;;) {
        asm volatile("" ::: "memory");
        u32 aa = 0xffffffffu, oo = 0u;
#pragma unroll
        for (int i = 0; i < 4; ++i) {
          a[i] = __builtin_amdgcn_raw_buffer_load_b128(rW, o0, i * 3072, 16);
          b[i] = __builtin_amdgcn_raw_buffer_load_b128(rW, o1, i * 3072, 16);
        }
#pragma unroll
        for (int i = 0; i < 4; ++i) {
          aa &= a[i][0] & a[i][1] & a[i][2] & a[i][3] & b[i][0] & b[i][1] & b[i][2] & b[i][3];
          oo |= a[i][0] | a[i][1] | a[i][2] | a[i][3] | b[i][0] | b[i][1] | b[i][2] | b[i][3];
        }
        bool ok = (aa & 1u) == par && (oo & 1u) == par;
        if (__all(ok)) break;
        if (*pflag == ep) { got = false; break; }
        if (dead || spin_fail(spins, ctl)) { dead = true; break; }
      }
      STAMP(5);
      if (got) {
      const float scale = 0.07216878364870322f;
      float sc[8];
#pragma unroll
      for (int k = 0; k < 4; ++k) {
        float x = (__uint_as_float(a[0][k]) + __uint_as_float(a[1][k])) + (__uint_as_float(a[2][k]) + __uint_as_float(a[3][k]));
        float y = (__uint_as_float(b[0][k]) + __uint_as_float(b[1][k])) + (__uint_as_float(b[2][k]) + __uint_as_float(b[3][k]));
        sc[k] = rbf(rbf(x) * scale); sc[4 + k] = rbf(rbf(y) * scale);
      }
      float mx = fmaxf(fmaxf(fmaxf(sc[0], sc[1]), fmaxf(sc[2], sc[3])), fmaxf(fmaxf(sc[4], sc[5]), fmaxf(sc[6], sc[7])));
      mx = fmaxf(mx, __uint_as_float(__builtin_amdgcn_update_dpp(0, __float_as_uint(mx), 0x128, 0xf, 0xf, false)));
      mx = red16_max(mx); mx = red32_max(mx);
      float sum = 0.f, sum2 = 0.f;
#pragma unroll
      for (int k = 0; k < 4; ++k) { sc[k] = __expf(sc[k] - mx); sum += sc[k]; sc[4 + k] = __expf(sc[4 + k] - mx); sum2 += sc[4 + k]; }
      if (q < 2) sum += sum2;
      sum += __uint_as_float(__builtin_amdgcn_update_dpp(0, __float_as_uint(sum), 0x128, 0xf, 0xf, false));
      sum = red16_add(sum); sum = red32_add(sum);
      float inv = __builtin_amdgcn_rcpf(sum);
      *(u32x2*)(pL + r * PST + q * 4) = (u32x2){pack2(sc[0] * inv, sc[1] * inv), pack2(sc[2] * inv, sc[3] * inv)};
      if (q < 2) *(u32x2*)(pL + r * PST + 32 + q * 4) = (u32x2){pack2(sc[4] * inv, sc[5] * inv), pack2(sc[6] * inv, sc[7] * inv)};
      asm volatile("s_waitcnt lgkmcnt(0)" ::: "memory");
      if (lane == 0) *pflag = ep;
      }
      STAMP(6);
    } else if (wave == 9) {
      u32x4 v[6]; u32 spins = 0;
      for (int i = 0; i < DLY_V; ++i) __builtin_amdgcn_s_sleep(4);
      for (;;) {
        asm volatile("" ::: "memory");
        bool ok = true;
#pragma unroll
        for (int i = 0; i < 6; ++i) { int id = lane + 64 * i; int src = id / 96, u = id % 96; v[i] = ld_sc1_16(rW, WS_GB + (u32)((h * 4 + src) * 192 + 96 + u) * 16); }
#pragma unroll
        for (int i = 0; i < 6; ++i) ok = ok && v[i][1] == ep && v[i][3] == ep;
        if (__all(ok)) break;
        if (dead || spin_fail(spins, ctl)) { dead = true; break; }
      }
#pragma unroll
      for (int i = 0; i < 6; ++i) {
        int id = lane + 64 * i; int src = id / 96, u = id % 96; int vt = u >> 5, rr = (u >> 2) & 7, gg = u & 3;
        *(u32x2*)(vL + rr * VST + src * 48 + vt * 16 + gg * 4) = (u32x2){v[i][0], v[i][2]};
      }
      asm volatile("s_waitcnt lgkmcnt(0)" ::: "memory");
      if (lane == 0) *vflag = ep;
    }
    if (wave < 10) {
      while (*vflag != ep) { if (dead) break; }
      asm volatile("" ::: "memory");
      f32x4 am = {0.f, 0.f, 0.f, 0.f};
      const u16* vb = vL + (fr & 7) * VST + g * 8;
#pragma unroll
      for (int t = 0; t < 6; ++t) am = mfma16(*(const bf16x8*)(vb + t * 32), Wof[t], am);
      u32x4 a0u = {vwx[0], vwx[1], vwx[2], vwx[3]};
      u32x4 a1u = {g < 2 ? pack2(am[0], am[1]) : 0u, g < 2 ? pack2(am[2], am[3]) : 0u, 0u, 0u};
      while (*pflag != ep) { if (dead) break; }
      asm volatile("s_waitcnt vmcnt(0)" ::: "memory");
      const u16* pr = pL + (fr & 7) * PST;
      u32x2 x0 = *(const u32x2*)(pr + 8 + g * 4), x1 = *(const u32x2*)(pr + 24 + g * 4), x2 = *(const u32x2*)(pr + (g & 1) * 4);
      u32x4 b0u = {x0[0], x0[1], x1[0], x1[1]};
      u32x4 b1u = {g < 2 ? x2[0] : 0u, g < 2 ? x2[1] : 0u, 0u, 0u};
      f32x4 acc = {0.f, 0.f, 0.f, 0.f};
      acc = mfma16(__builtin_bit_cast(bf16x8, a0u), __builtin_bit_cast(bf16x8, b0u), acc);
      acc = mfma16(__builtin_bit_cast(bf16x8, a1u), __builtin_bit_cast(bf16x8, b1u), acc);
      if (fr < 8) {
        u32 o = WS_GA1 + (u32)(((j * 10 + wave) * 32 + h) * 32 + fr * 4 + g) * 16;
        st_sc1_16(rW, o, (u32x4){(__float_as_uint(acc[0]) & ~1u) | par, (__float_as_uint(acc[1]) & ~1u) | par,
                                 (__float_as_uint(acc[2]) & ~1u) | par, (__float_as_uint(acc[3]) & ~1u) | par});
      }
    }
    STAMP(8);
    if (wave == 10) asm volatile("s_waitcnt vmcnt(0)" ::: "memory");
    lbar();
    if ((s & 7) == 7) TL(cu, 3 + (s >> 3));
    if ((s & 7) == 7 && tid == 0) __hip_atomic_store((u32*)(ws + WS_FCH + (u32)cu * 64), (u32)(s >> 3) + 1u, RLX_AGENT);
    if (wave < 10) calc_vwx(buf ^ 1, fr, g);
    if (wave >= NPOLL) {
      if (cu < 80) {
        const int ch = lane & 15, hq = lane >> 4;
        bool got = true;
        for (int i = 0; i < DLY_A1; ++i) __builtin_amdgcn_s_sleep(4);
        const u32 rbase = WS_GA1 + (u32)(((cu >> 1) * 32 + hq * 8) * 32 + (cu & 1) * 16 + ch) * 16;
        u32x4 a[8]; u32 spins = 0;
        for (;;) {
          asm volatile("" ::: "memory");
          u32 aa = 0xffffffffu, oo = 0u;
#pragma unroll
          for (int i = 0; i < 8; ++i) a[i] = __builtin_amdgcn_raw_buffer_load_b128(rW, rbase, i * 512, 16);
#pragma unroll
          for (int i = 0; i < 8; ++i) { aa &= a[i][0] & a[i][1] & a[i][2] & a[i][3]; oo |= a[i][0] | a[i][1] | a[i][2] | a[i][3]; }
          bool ok = (aa & 1u) == par && (oo & 1u) == par;
          if (__all(ok)) break;
          if (*rflag == ep) { got = false; break; }
          if (dead || spin_fail(spins, ctl)) { dead = true; break; }
        }
        STAMP(9);
        if (got) {
        float t[4];
#pragma unroll
        for (int k = 0; k < 4; ++k) {
          float x = ((__uint_as_float(a[0][k]) + __uint_as_float(a[1][k])) + (__uint_as_float(a[2][k]) + __uint_as_float(a[3][k])))
                  + ((__uint_as_float(a[4][k]) + __uint_as_float(a[5][k])) + (__uint_as_float(a[6][k]) + __uint_as_float(a[7][k])));
          x = red16_add(x); x = red32_add(x); t[k] = x;
        }
        u32 w0 = pack2(t[0], t[1]), w1 = pack2(t[2], t[3]);
#pragma unroll
        for (int it = 0; it < A2_REP / 4; ++it) st_sc1_16(rW, WS_GA2 + (u32)(cu * 16 + ch) * 16 + (u32)(hq + 4 * it) * A2_STRIDE, (u32x4){w0, ep, w1, ep});
        if (lane == 0) *rflag = ep;
        }
        STAMP(10);
      }
      else if (wave == 11) {
        u32 spins = 0;
        for (int i = 0; i < DLY_S; ++i) __builtin_amdgcn_s_sleep(4);
        for (;;) {
          asm volatile("" ::: "memory");
          u32x4 a = ld_sc1_16(rW, a2base + (u32)(cu - 80) * 256);
          if (__all(a[1] == ep)) break;
          if (dead || spin_fail(spins, ctl)) { dead = true; break; }
        }
      }
    }
    lbar();
  }
  TL(cu, 10);
  asm volatile("s_waitcnt vmcnt(0)" ::: "memory");
  __syncthreads();
  if (tid == 0) __hip_atomic_store((u32*)(ws + WS_FCH + (u32)cu * 64), 8u, RLX_AGENT);
  phase_b<true>(7, cu, ws, rW, smem);
  TL(cu, 11);
}

__global__ __launch_bounds__(NTH, 3) void k_main(FP p) {
  extern __shared__ __attribute__((aligned(16))) char smem[];
  const int b = blockIdx.x;
  const int id = (b >> 4) * 8 + (b & 7);
  if ((b >> 3) & 1) gemm_role(id, smem);
  else chain_role(id, smem);
}

extern "C" void kernel_launch(void* const* d_in, const int* in_sizes, int n_in,
                              void* d_out, int out_size, void* d_ws, size_t ws_size,
                              hipStream_t stream) {
  char* ws = (char*)d_ws;
  static bool attr_done = false;
  if (!attr_done) {
    (void)hipFuncSetAttribute((const void*)k_main, hipFuncAttributeMaxDynamicSharedMemorySize, LDS_BYTES);
    attr_done = true;
  }
  k_ln<<<S_ / 4, 256, 0, stream>>>((const u16*)d_in[0], (u16*)(ws + WS_XN), (u32x4*)ws);
  FP p{(const u16*)d_in[0], (const u16*)d_in[1], (const u16*)d_in[2], (const u16*)d_in[3], (const u16*)d_in[4], (const u16*)d_in[5],
       (const u16*)d_in[8], (const u16*)d_in[9], (const u16*)d_in[10], (u16*)d_out, ws};
  k_main<<<256, NTH, LDS_BYTES, stream>>>(p);
}
```

```cpp
#include <hip/hip_runtime.h>
#include <hip/hip_bf16.h>
#include <stdint.h>

typedef unsigned short u16;
typedef unsigned int u32;
typedef unsigned long long u64;
typedef short bf16x8 __attribute__((ext_vector_type(8)));
typedef short bf16x4 __attribute__((ext_vector_type(4)));
typedef float f32x4 __attribute__((ext_vector_type(4)));
typedef u32 u32x4 __attribute__((ext_vector_type(4)));
typedef u32 u32x2 __attribute__((ext_vector_type(2)));
#define LAS __attribute__((address_space(3)))
#define RLX_AGENT __ATOMIC_RELAXED, __HIP_MEMORY_SCOPE_AGENT

constexpr int S_ = 2048, D_ = 640, H_ = 32, E_ = 192, HE_ = H_ * E_;
constexpr int M_ = 8, SEG_ = 32, NSEG_ = 64, F_ = 1536;

constexpr u32 WS_CTL   = 0;
constexpr u32 WS_FQKV  = 4096;
constexpr u32 WS_FCH   = WS_FQKV + 8 * 96 * 64;
constexpr u32 WS_FAT   = WS_FCH + 128 * 64;
constexpr u32 WS_FOP   = WS_FAT + 64 * 64;
constexpr u32 WS_FLN   = WS_FOP + 8 * 64;
constexpr u32 WS_FGL   = WS_FLN + 8 * 64;
constexpr u32 WS_GA2   = (WS_FGL + 8 * 64 + 4095) / 4096 * 4096;
#define A2R 8
constexpr int A2_REP = A2R, A2_STRIDE = 20736;
#define RNG 8
constexpr u32 A2_SZ = A2_REP * A2_STRIDE, GB_SZ = 128 * 3072, A1_SZ = 40 * 32 * 512;
constexpr u32 WS_GB0   = WS_GA2 + RNG * A2_SZ;
constexpr u32 WS_GA10  = WS_GB0 + RNG * GB_SZ;
constexpr u32 WS_ZEND  = WS_GA10 + RNG * A1_SZ;
constexpr u32 WS_XN    = (WS_ZEND + 4095) / 4096 * 4096;
constexpr u32 WS_KX    = WS_XN + S_ * D_ * 2;
constexpr u32 WS_VX    = WS_KX + S_ * HE_ * 2;
constexpr u32 WS_QX    = WS_VX + S_ * HE_ * 2;
constexpr u32 WS_KM    = WS_QX + S_ * HE_ * 2;
constexpr u32 WS_VM    = WS_KM + NSEG_ * M_ * HE_ * 2;
constexpr u32 WS_OX    = WS_VM + NSEG_ * M_ * HE_ * 2;
constexpr u32 WS_H     = WS_OX + S_ * HE_ * 2;
constexpr u32 WS_HN    = WS_H + S_ * D_ * 2;
constexpr u32 WS_GU    = WS_HN + S_ * D_ * 2;
constexpr u32 WS_SLAB  = WS_GU + S_ * F_ * 2;
constexpr int NSLAB = 12;
constexpr u32 WS_END   = WS_SLAB + (u32)NSLAB * S_ * D_ * 4;

__device__ __forceinline__ float bf2f(u16 v) { return __uint_as_float(((u32)v) << 16); }
__device__ __forceinline__ u16 f2bf(float f) {
  __hip_bfloat16 b = __float2bfloat16(f);
  return *reinterpret_cast<u16*>(&b);
}
__device__ __forceinline__ float rbf(float f) { return bf2f(f2bf(f)); }
__device__ __forceinline__ u32 pack2(float a, float b) { return (u32)f2bf(a) | ((u32)f2bf(b) << 16); }
__device__ __forceinline__ f32x4 mfma16(bf16x8 a, bf16x8 b, f32x4 c) {
  return __builtin_amdgcn_mfma_f32_16x16x32_bf16(a, b, c, 0, 0, 0);
}
__device__ __forceinline__ bf16x4 tr_read(const void* p) {
  return __builtin_amdgcn_ds_read_tr16_b64_v4i16((LAS bf16x4*)p);
}
__device__ __forceinline__ bf16x8 cat8(bf16x4 a, bf16x4 b) {
  bf16x8 r; r[0]=a[0]; r[1]=a[1]; r[2]=a[2]; r[3]=a[3]; r[4]=b[0]; r[5]=b[1]; r[6]=b[2]; r[7]=b[3]; return r;
}
__device__ __forceinline__ void lbar() { asm volatile("s_waitcnt lgkmcnt(0)\n\ts_barrier" ::: "memory"); }
typedef __amdgpu_buffer_rsrc_t rsrc_t;
__device__ __forceinline__ u32x4 ld_sc1_16(rsrc_t r, u32 off) { return __builtin_amdgcn_raw_buffer_load_b128(r, off, 0, 16); }
__device__ __forceinline__ void st_sc1_16(rsrc_t r, u32 off, u32x4 v) { __builtin_amdgcn_raw_buffer_store_b128(v, r, off, 0, 16); }
__device__ __forceinline__ void st_sc1_8(rsrc_t r, u32 off, u32x2 v) { __builtin_amdgcn_raw_buffer_store_b64(v, r, off, 0, 16); }
__device__ __forceinline__ float red16_max(float x) {
  auto r = __builtin_amdgcn_permlane16_swap(__float_as_uint(x), __float_as_uint(x), false, false);
  return fmaxf(__uint_as_float(r[0]), __uint_as_float(r[1]));
}
__device__ __forceinline__ float red32_max(float x) {
  auto r = __builtin_amdgcn_permlane32_swap(__float_as_uint(x), __float_as_uint(x), false, false);
  return fmaxf(__uint_as_float(r[0]), __uint_as_float(r[1]));
}
__device__ __forceinline__ float red16_add(float x) {
  auto r = __builtin_amdgcn_permlane16_swap(__float_as_uint(x), __float_as_uint(x), false, false);
  return __uint_as_float(r[0]) + __uint_as_float(r[1]);
}
__device__ __forceinline__ float red32_add(float x) {
  auto r = __builtin_amdgcn_permlane32_swap(__float_as_uint(x), __float_as_uint(x), false, false);
  return __uint_as_float(r[0]) + __uint_as_float(r[1]);
}

constexpr u32 SPIN_CAP = 1u << 18;
__device__ __forceinline__ bool spin_fail(u32& spins, u32* ctl) {
  ++spins;
  if (spins > SPIN_CAP) { __hip_atomic_store(ctl, 1u, RLX_AGENT); return true; }
  return (spins & 255u) == 0u && __builtin_amdgcn_readfirstlane(__hip_atomic_load(ctl, RLX_AGENT)) != 0u;
}

struct FP {
  const u16* x; const u16* mem0; const u16* Wq; const u16* Wk; const u16* Wv; const u16* Wo;
  const u16* wg; const u16* wu; const u16* wd; u16* out; char* ws;
};
typedef const __attribute__((address_space(4))) FP* kargp_t;
__device__ __forceinline__ FP getp() {
#if defined(__HIP_DEVICE_COMPILE__)
  kargp_t kp = (kargp_t)__builtin_amdgcn_kernarg_segment_ptr(); asm volatile("" : "+s"(kp)); return *kp;
#else
  return FP{};
#endif
}
constexpr int NTH = 768;
constexpr int LDS_BYTES = 153600;

__device__ __forceinline__ void ln_row(const float (&v)[10], u32 (&o)[5]) {
  float sum = 0.f;
#pragma unroll
  for (int i = 0; i < 10; ++i) sum += v[i];
#pragma unroll
  for (int m = 32; m >= 1; m >>= 1) sum += __shfl_xor(sum, m);
  float mean = sum * (1.0f / D_);
  float sq = 0.f;
#pragma unroll
  for (int i = 0; i < 10; ++i) { float d = v[i] - mean; sq += d * d; }
#pragma unroll
  for (int m = 32; m >= 1; m >>= 1) sq += __shfl_xor(sq, m);
  float var = sq * (1.0f / D_);
  float mb = rbf(mean), vb = rbf(var);
  float den = rbf(sqrtf(rbf(vb + 1e-5f)));
#pragma unroll
  for (int i = 0; i < 5; ++i) o[i] = pack2(rbf(v[2 * i] - mb) / den, rbf(v[2 * i + 1] - mb) / den);
}
__global__ __launch_bounds__(256) void k_ln(const u16* __restrict__ x, u16* __restrict__ y, u32x4* __restrict__ zero) {
  for (u32 i = blockIdx.x * 256 + threadIdx.x; i < WS_ZEND / 16; i += (S_ / 4) * 256) zero[i] = (u32x4){0u, 0u, 0u, 0u};
  int row = blockIdx.x * 4 + (threadIdx.x >> 6);
  int lane = threadIdx.x & 63;
  const u32* xr = (const u32*)(x + (size_t)row * D_);
  float v[10];
#pragma unroll
  for (int i = 0; i < 5; ++i) { u32 w = xr[i * 64 + lane]; v[2 * i] = bf2f((u16)(w & 0xffff)); v[2 * i + 1] = bf2f((u16)(w >> 16)); }
  u32 o[5]; ln_row(v, o);
  u32* yr = (u32*)(y + (size_t)row * D_);
#pragma unroll
  for (int i = 0; i < 5; ++i) yr[i * 64 + lane] = o[i];
}

__device__ __forceinline__ void wait_flag(char* ws, u32 foff, u32 target) {
  if (threadIdx.x == 0) {
    u32* f = (u32*)(ws + foff); u32* ctl = (u32*)(ws + WS_CTL); u32 spins = 0;
    while (__hip_atomic_load(f, RLX_AGENT) < target) {
      __builtin_amdgcn_s_sleep(8);
      if (++spins > (1u << 20)) { __hip_atomic_store(ctl, 1u, RLX_AGENT); break; }
      if ((spins & 63u) == 0u && __hip_atomic_load(ctl, RLX_AGENT) != 0u) break;
    }
  }
}
__device__ __forceinline__ void acquire_bar() {
  if (threadIdx.x == 0) { __builtin_amdgcn_fence(__ATOMIC_ACQUIRE, "agent"); asm volatile("s_waitcnt vmcnt(0)" ::: "memory"); }
  __syncthreads();
}
__device__ __forceinline__ void signal_flag(char* ws, u32 foff) {
  asm volatile("s_waitcnt vmcnt(0)" ::: "memory");
  __syncthreads();
  if (threadIdx.x == 0) __hip_atomic_fetch_add((u32*)(ws + foff), 1u, RLX_AGENT);
}

#define TLMB 3
#define TL(blk, i) do {} while (0)
template <int NWN> __device__ __forceinline__ int bswz(int row) {
  if (NWN == 3) return (((row >> 1) & 1) | (((row >> 3) & 1) << 1)) << 1;
  else if (NWN == 2) return ((row & 3) << 2) | ((row >> 2) & 3);
  else return (((row >> 1) & 1) | (((row >> 3) & 1) << 1)) << 1;
}
enum { E_BF16 = 0, E_SLAB = 1, E_GLU = 2, E_DOWN = 3 };
template <int NWM, int NWN, int KG, int EPI>
__device__ __forceinline__ void gtile(const u16* __restrict__ A, int lda, const u16* __restrict__ B0, const u16* __restrict__ B1, int ldb,
                                      int nk, int m0, int nb, rsrc_t rW, u32 coff, int ldc, const u16* resid, u16* outp, char* smem) {
  constexpr int BM = NWM * 64, BN = NWN * 64, BROW = BN * 2, NA = BM / 16, NI = NA + BN / 16, NT = NI * KG, ST_BYTES = NT * 1024;
  constexpr int NST = (143360 / ST_BYTES) < 16 ? (143360 / ST_BYTES) : 16, PD = NST - 1;
  constexpr int NPW = (NT + 11) / 12;
  constexpr int NW = NWM * NWN;
  int tz = threadIdx.x; asm volatile("" : "+v"(tz));
  const int tid = tz, lane = tid & 63, wave = __builtin_amdgcn_readfirstlane(tid >> 6);
  const int kg = wave / NW, wv = wave % NW, wm = wv % NWM, wn = wv / NWM;
  const int fr = lane & 15, g = lane >> 4;
  const u16* src[NPW]; u32 sstep[NPW];
#pragma unroll
  for (int i = 0; i < NPW; ++i) {
    int it = wave + 12 * i; if (it >= NT) it = 0;
    int sub = it / NI, ii = it % NI;
    if (ii < NA) { int b = ii * 1024 + lane * 16; int row = b >> 6, pos = (b >> 4) & 3, ch = pos ^ ((4 - (row >> 2)) & 3);
      src[i] = A + (size_t)(m0 + row) * lda + ch * 8 + sub * 32; sstep[i] = 32 * KG; }
    else { int b = (ii - NA) * 1024 + lane * 16; int row = b / BROW, pos = (b % BROW) >> 4;
      int ch = pos ^ bswz<NWN>(row);
      if (EPI == E_GLU) { int w2 = ch >> 3, within = ch & 7; const u16* base = (within >> 2) ? B1 : B0;
        src[i] = base + (size_t)(row + sub * 32) * ldb + nb + w2 * 32 + (within & 3) * 8; }
      else src[i] = B0 + (size_t)(row + sub * 32) * ldb + nb + ch * 8;
      sstep[i] = 32u * KG * (u32)ldb; }
  }
  const int npw = (wave + 12 * (NPW - 1)) < NT ? NPW : NPW - 1;
  auto stage = [&](int kt, int buf) {
    char* sb = smem + buf * ST_BYTES;
#pragma unroll
    for (int i = 0; i < NPW; ++i)
      if (i < npw) __builtin_amdgcn_global_load_lds((const u32*)(src[i] + (size_t)kt * sstep[i]), (LAS u32*)(sb + (wave + 12 * i) * 1024), 16, 0, 0);
  };
  f32x4 acc[4][4];
#pragma unroll
  for (int a = 0; a < 4; ++a)
#pragma unroll
    for (int b = 0; b < 4; ++b) acc[a][b] = (f32x4){0.f, 0.f, 0.f, 0.f};
  const bool compk = wave < NW * KG;
  const bool comp = wave < NW;
  const int nst = nk * 2 / KG;
#pragma unroll 1
  for (int i = 0; i < PD; ++i) if (i < nst) stage(i, i);
  int cb = 0, pb = PD;
  const int q = fr >> 2, pp = fr & 3;
  const int r0 = g * 8 + q, r1 = r0 + 4;
  const int x0 = bswz<NWN>(r0), x1 = bswz<NWN>(r1);
  const int aoff = kg * NI * 1024 + (wm * 64 + fr) * 64 + ((g ^ ((4 - (fr >> 2)) & 3)) << 4);
#pragma unroll 1
  for (int kt = 0; kt < nst; ++kt) {
    if (kt + PD <= nst) {
      if (npw == NPW) asm volatile("s_waitcnt vmcnt(%0)" :: "i"(NPW * (PD - 1)) : "memory");
      else asm volatile("s_waitcnt vmcnt(%0)" :: "i"((NPW - 1) * (PD - 1)) : "memory");
    }
    else asm volatile("s_waitcnt vmcnt(0)" ::: "memory");
    lbar();
    if (kt + PD < nst) stage(kt + PD, pb);
    if (compk) {
      const char* sa = smem + cb * ST_BYTES; const char* sb = sa + (kg * NI + NA) * 1024;
      bf16x8 af[4], bfr[4];
#pragma unroll
      for (int a = 0; a < 4; ++a) af[a] = *(const bf16x8*)(sa + aoff + a * 1024);
#pragma unroll
      for (int b = 0; b < 4; ++b) {
        int ch = wn * 8 + b * 2 + (pp >> 1);
        bfr[b] = cat8(tr_read(sb + r0 * BROW + ((ch ^ x0) << 4) + 8 * (pp & 1)), tr_read(sb + r1 * BROW + ((ch ^ x1) << 4) + 8 * (pp & 1)));
      }
#pragma unroll
      for (int a = 0; a < 4; ++a)
#pragma unroll
        for (int b = 0; b < 4; ++b) acc[a][b] = mfma16(bfr[b], af[a], acc[a][b]);
    }
    cb = cb == NST - 1 ? 0 : cb + 1; pb = pb == NST - 1 ? 0 : pb + 1;
  }
  if (KG > 1) {
    lbar();
    if (compk && kg > 0) {
      f32x4* d = (f32x4*)(smem + (wave - NW) * 16384) + lane;
#pragma unroll
      for (int a = 0; a < 4; ++a)
#pragma unroll
        for (int b = 0; b < 4; ++b) d[(a * 4 + b) * 64] = acc[a][b];
    }
    lbar();
    if (comp) {
#pragma unroll
      for (int k2 = 1; k2 < KG; ++k2) {
        const f32x4* d = (const f32x4*)(smem + ((k2 - 1) * NW + wv) * 16384) + lane;
#pragma unroll
        for (int a = 0; a < 4; ++a)
#pragma unroll
          for (int b = 0; b < 4; ++b) acc[a][b] += d[(a * 4 + b) * 64];
      }
    }
  }
  if (EPI == E_DOWN) {
    if (comp) {
#pragma unroll
      for (int a = 0; a < 4; ++a) {
        const int m = m0 + wm * 64 + a * 16 + fr;
#pragma unroll
        for (int b = 0; b < 4; ++b) {
          int n = nb + wn * 64 + b * 16 + g * 4;
          u32x2 rv = *(const u32x2*)(resid + (size_t)m * ldc + n);
          float o0 = rbf(acc[a][b][0]) + bf2f((u16)(rv[0] & 0xffff)), o1 = rbf(acc[a][b][1]) + bf2f((u16)(rv[0] >> 16));
          float o2 = rbf(acc[a][b][2]) + bf2f((u16)(rv[1] & 0xffff)), o3 = rbf(acc[a][b][3]) + bf2f((u16)(rv[1] >> 16));
          *(u32x2*)(outp + (size_t)m * ldc + n) = (u32x2){pack2(o0, o1), pack2(o2, o3)};
        }
      }
    }
  } else {
    constexpr int OC = EPI == E_GLU ? NWN * 32 : BN;
    constexpr int ES = EPI == E_SLAB ? 4 : 2;
    constexpr int RB = OC * ES, RS = RB + 16, CPR = RB / 16;
    lbar();
    if (comp) {
#pragma unroll
      for (int a = 0; a < 4; ++a) {
        char* rowp = smem + (wm * 64 + a * 16 + fr) * RS;
        if (EPI == E_GLU) {
#pragma unroll
          for (int b = 0; b < 2; ++b) {
            float o[4];
#pragma unroll
            for (int r = 0; r < 4; ++r) o[r] = rbf(acc[a][b][r]) * rbf(acc[a][b + 2][r]);
            *(u32x2*)(rowp + (wn * 32 + b * 16 + g * 4) * 2) = (u32x2){pack2(o[0], o[1]), pack2(o[2], o[3])};
          }
        } else {
#pragma unroll
          for (int b = 0; b < 4; ++b) {
            if (EPI == E_BF16) *(u32x2*)(rowp + (wn * 64 + b * 16 + g * 4) * 2) = (u32x2){pack2(acc[a][b][0], acc[a][b][1]), pack2(acc[a][b][2], acc[a][b][3])};
            else *(f32x4*)(rowp + (wn * 64 + b * 16 + g * 4) * 4) = acc[a][b];
          }
        }
      }
    }
    lbar();
#pragma unroll 2
    for (int c = tid; c < BM * CPR; c += NTH) {
      int row = c / CPR, ch = c % CPR;
      u32x4 v = *(const u32x4*)(smem + row * RS + ch * 16);
      st_sc1_16(rW, coff + (u32)((m0 + row) * ldc + nb) * ES + ch * 16, v);
    }
  }
}

__device__ __forceinline__ void attn_wave(const char* ws, rsrc_t rW, int s, int h, u16* vL) {
  int tz = threadIdx.x; asm volatile("" : "+v"(tz));
  const int lane = tz & 63, fr = lane & 15, g = lane >> 4, q4 = fr >> 2, p4 = fr & 3;
  const u16* qx = (const u16*)(ws + WS_QX); const u16* kx = (const u16*)(ws + WS_KX); const u16* vx = (const u16*)(ws + WS_VX);
  const u16* km = (const u16*)(ws + WS_KM); const u16* vm = (const u16*)(ws + WS_VM);
#pragma unroll
  for (int i = 0; i < 16; ++i) {
    int c = i * 64 + lane; int row = c / 25, ch = c % 25; if (row > 39) row = 39; if (ch > 23) ch = 23;
    const u16* sp = row < 8 ? vm + ((size_t)(s * M_ + row)) * HE_ : vx + ((size_t)(s * SEG_ + row - 8)) * HE_;
    __builtin_amdgcn_global_load_lds((const u32*)(sp + h * E_ + ch * 8), (LAS u32*)((char*)vL + i * 1024), 16, 0, 0);
  }
  f32x4 sc[3][2];
#pragma unroll
  for (int a = 0; a < 3; ++a) { sc[a][0] = (f32x4){0.f, 0.f, 0.f, 0.f}; sc[a][1] = (f32x4){0.f, 0.f, 0.f, 0.f}; }
  {
    const u16* kp[3];
#pragma unroll
    for (int a = 0; a < 3; ++a) {
      int key = a * 16 + fr; if (key > 39) key = 39;
      kp[a] = (key < 8 ? km + ((size_t)(s * M_ + key)) * HE_ : kx + ((size_t)(s * SEG_ + key - 8)) * HE_) + h * E_ + g * 8;
    }
    const u16* qp0 = qx + ((size_t)(s * SEG_ + fr)) * HE_ + h * E_ + g * 8;
    const u16* qp1 = qp0 + (size_t)16 * HE_;
#pragma unroll
    for (int hf = 0; hf < 2; ++hf) {
      bf16x8 qf[3][2], kf[3][3];
#pragma unroll
      for (int kk = 0; kk < 3; ++kk) {
        qf[kk][0] = *(const bf16x8*)(qp0 + (hf * 3 + kk) * 32); qf[kk][1] = *(const bf16x8*)(qp1 + (hf * 3 + kk) * 32);
#pragma unroll
        for (int a = 0; a < 3; ++a) kf[kk][a] = *(const bf16x8*)(kp[a] + (hf * 3 + kk) * 32);
      }
#pragma unroll
      for (int kk = 0; kk < 3; ++kk)
#pragma unroll
        for (int a = 0; a < 3; ++a) { sc[a][0] = mfma16(kf[kk][a], qf[kk][0], sc[a][0]); sc[a][1] = mfma16(kf[kk][a], qf[kk][1], sc[a][1]); }
    }
  }
  const float scale = 0.07216878364870322f;
  bf16x8 pb[2][2];
#pragma unroll
  for (int qt = 0; qt < 2; ++qt) {
    int qrow = qt * 16 + fr;
    float v[12]; float mx = -3.0e38f;
#pragma unroll
    for (int a = 0; a < 3; ++a)
#pragma unroll
      for (int r = 0; r < 4; ++r) {
        int key = a * 16 + g * 4 + r;
        float x = rbf(rbf(sc[a][qt][r]) * scale);
        bool vis = key < 8 || (key - 8) <= qrow;
        x = vis ? x : rbf(-1000000000.0f);
        v[a * 4 + r] = x;
        if (key < 40) mx = fmaxf(mx, x);
      }
    mx = red16_max(mx); mx = red32_max(mx);
    float sum = 0.f;
#pragma unroll
    for (int a = 0; a < 3; ++a)
#pragma unroll
      for (int r = 0; r < 4; ++r) {
        int key = a * 16 + g * 4 + r;
        float e = key < 40 ? __expf(v[a * 4 + r] - mx) : 0.f;
        v[a * 4 + r] = e; sum += e;
      }
    sum = red16_add(sum); sum = red32_add(sum);
    float inv = 1.0f / sum;
#pragma unroll
    for (int r = 0; r < 4; ++r) {
      pb[qt][0][r] = (short)f2bf(v[r] * inv); pb[qt][0][4 + r] = (short)f2bf(v[4 + r] * inv);
      pb[qt][1][r] = (short)f2bf(v[8 + r] * inv); pb[qt][1][4 + r] = 0;
    }
  }
  asm volatile("s_waitcnt vmcnt(0)" ::: "memory");
#pragma unroll
  for (int i = 0; i < 4; ++i) { int c = i * 64 + lane; if (c < 200) *(u32x4*)(vL + 40 * 200 + c * 8) = (u32x4){0u, 0u, 0u, 0u}; }
  asm volatile("s_waitcnt lgkmcnt(0)" ::: "memory");
  u32x2 ov[12][2];
#pragma unroll
  for (int et = 0; et < 12; ++et) {
    int r0 = g * 4 + q4;
    bf16x8 a0 = cat8(tr_read(vL + r0 * 200 + et * 16 + p4 * 4), tr_read(vL + (16 + r0) * 200 + et * 16 + p4 * 4));
    bf16x4 t2 = tr_read(vL + (32 + r0) * 200 + et * 16 + p4 * 4);
    bf16x8 a1 = cat8(t2, t2);
#pragma unroll
    for (int qt = 0; qt < 2; ++qt) {
      f32x4 acc = {0.f, 0.f, 0.f, 0.f};
      acc = mfma16(a0, pb[qt][0], acc);
      acc = mfma16(a1, pb[qt][1], acc);
      ov[et][qt] = (u32x2){pack2(acc[0], acc[1]), pack2(acc[2], acc[3])};
    }
  }
  asm volatile("s_waitcnt lgkmcnt(0)" ::: "memory");
#pragma unroll
  for (int et = 0; et < 12; ++et)
#pragma unroll
    for (int qt = 0; qt < 2; ++qt) *(u32x2*)(vL + (qt * 16 + fr) * 200 + et * 16 + g * 4) = ov[et][qt];
  asm volatile("s_waitcnt lgkmcnt(0)" ::: "memory");
#pragma unroll
  for (int i = 0; i < 12; ++i) {
    int c = i * 64 + lane; int row = c / 24, ch = c % 24;
    u32x4 v = *(const u32x4*)(vL + row * 200 + ch * 8);
    st_sc1_16(rW, WS_OX + (u32)((s * SEG_ + row) * HE_ + h * E_ + ch * 8) * 2, v);
  }
}

template <int NS>
__device__ __forceinline__ void redln_row(char* ws, rsrc_t rW, int row) {
  const FP p = getp();
  const int lane = threadIdx.x & 63;
  const bool two = lane < 16;
  float v[16];
#pragma unroll
  for (int hf = 0; hf < 2; ++hf) {
    int c0 = (hf == 0 ? lane : (two ? 64 + lane : lane)) * 8;
    const f32x4* sl = (const f32x4*)((const float*)(ws + WS_SLAB) + (size_t)row * D_ + c0);
    f32x4 lo[NS], hi[NS];
#pragma unroll
    for (int q = 0; q < NS; ++q) { lo[q] = sl[(size_t)q * (S_ * D_ / 4)]; hi[q] = sl[(size_t)q * (S_ * D_ / 4) + 1]; }
    f32x4 s0 = ((lo[0] + lo[1]) + (lo[2] + lo[3])) + ((lo[4] + lo[5]) + (lo[6] + lo[7]));
    f32x4 s1 = ((hi[0] + hi[1]) + (hi[2] + hi[3])) + ((hi[4] + hi[5]) + (hi[6] + hi[7]));
    if (NS == 12) { s0 += (lo[8 % NS] + lo[9 % NS]) + (lo[10 % NS] + lo[11 % NS]); s1 += (hi[8 % NS] + hi[9 % NS]) + (hi[10 % NS] + hi[11 % NS]); }
    u32x4 xw = *(const u32x4*)(p.x + (size_t)row * D_ + c0);
    u32x4 hw;
#pragma unroll
    for (int i = 0; i < 2; ++i) {
      hw[i] = pack2(rbf(s0[2 * i]) + bf2f((u16)(xw[i] & 0xffff)), rbf(s0[2 * i + 1]) + bf2f((u16)(xw[i] >> 16)));
      hw[2 + i] = pack2(rbf(s1[2 * i]) + bf2f((u16)(xw[2 + i] & 0xffff)), rbf(s1[2 * i + 1]) + bf2f((u16)(xw[2 + i] >> 16)));
    }
#pragma unroll
    for (int i = 0; i < 4; ++i) { v[hf * 8 + 2 * i] = bf2f((u16)(hw[i] & 0xffff)); v[hf * 8 + 2 * i + 1] = bf2f((u16)(hw[i] >> 16)); }
    if (hf == 0 || two) st_sc1_16(rW, WS_H + (u32)(row * D_ + c0) * 2, hw);
  }
  float sum = 0.f;
#pragma unroll
  for (int i = 0; i < 8; ++i) sum += v[i] + (two ? v[8 + i] : 0.f);
#pragma unroll
  for (int m = 32; m >= 1; m >>= 1) sum += __shfl_xor(sum, m);
  float mean = sum * (1.0f / D_), sq = 0.f;
#pragma unroll
  for (int i = 0; i < 8; ++i) { float d = v[i] - mean, e = v[8 + i] - mean; sq += d * d + (two ? e * e : 0.f); }
#pragma unroll
  for (int m = 32; m >= 1; m >>= 1) sq += __shfl_xor(sq, m);
  float mbf = rbf(mean), den = rbf(sqrtf(rbf(rbf(sq * (1.0f / D_)) + 1e-5f)));
#pragma unroll
  for (int hf = 0; hf < 2; ++hf) {
    u32x4 o;
#pragma unroll
    for (int i = 0; i < 4; ++i) o[i] = pack2(rbf(v[hf * 8 + 2 * i] - mbf) / den, rbf(v[hf * 8 + 2 * i + 1] - mbf) / den);
    if (hf == 0 || two) st_sc1_16(rW, WS_HN + (u32)(row * D_ + (hf == 0 ? lane : 64 + lane) * 8) * 2, o);
  }
}

template <bool FINE>
__device__ __forceinline__ void phase_b(int mb, int gb, char* ws, rsrc_t rW, char* smem) {
  const int wave = __builtin_amdgcn_readfirstlane(threadIdx.x >> 6);
  if (FINE || gb < 32) {
    const int hh = gb & 31;
    wait_flag(ws, WS_FQKV + (u32)(mb * 96 + 64 + hh) * 64, 1);
#pragma unroll 1
    for (int j = 0; j < 4; ++j) wait_flag(ws, WS_FCH + (u32)(hh * 4 + j) * 64, (u32)(mb + 1));
    acquire_bar();
    if (mb == TLMB) TL((FINE ? 0 : 128) + gb, 16);
    if (wave < (FINE ? 2 : 8)) attn_wave(ws, rW, mb * 8 + (FINE ? (gb >> 5) * 2 : 0) + wave, hh, (u16*)(smem + wave * 19200));
    signal_flag(ws, WS_FAT + (u32)mb * 64);
    if (mb == TLMB) TL((FINE ? 0 : 128) + gb, 17);
  }
  if (FINE) {
    if (gb < 120) {
      int mi = gb & 1, nt = (gb >> 1) % 5, ks = gb / 10;
      wait_flag(ws, WS_FAT + (u32)mb * 64, 128);
      acquire_bar();
    if (mb == TLMB) TL((FINE ? 0 : 128) + gb, 18);
      const FP p = getp();
      gtile<2, 2, 2, E_SLAB>((const u16*)(ws + WS_OX) + ks * 512, HE_, p.Wo + (size_t)ks * 512 * D_, nullptr, D_, 8, mb * 256 + mi * 128, nt * 128, rW,
                             WS_SLAB + (u32)ks * (S_ * D_ * 4), D_, nullptr, nullptr, smem);
      signal_flag(ws, WS_FOP + (u32)mb * 64);
    if (mb == TLMB) TL((FINE ? 0 : 128) + gb, 19);
    }
  } else if (gb >= 32 && gb < 72) {
    int t = gb - 32, nt = t % 5, ks = t / 5;
    wait_flag(ws, WS_FAT + (u32)mb * 64, 32);
    acquire_bar();
    if (mb == TLMB) TL((FINE ? 0 : 128) + gb, 18);
    const FP p = getp();
    gtile<4, 2, 1, E_SLAB>((const u16*)(ws + WS_OX) + ks * 768, HE_, p.Wo + (size_t)ks * 768 * D_, nullptr, D_, 12, mb * 256, nt * 128, rW,
                           WS_SLAB + (u32)ks * (S_ * D_ * 4), D_, nullptr, nullptr, smem);
    signal_flag(ws, WS_FOP + (u32)mb * 64);
    if (mb == TLMB) TL((FINE ? 0 : 128) + gb, 19);
  }
  if (FINE || (gb >= 32 && gb < 54)) {
    wait_flag(ws, WS_FOP + (u32)mb * 64, FINE ? 120 : 40);
    acquire_bar();
    if (mb == TLMB) TL((FINE ? 0 : 128) + gb, 20);
    const int rr = FINE ? gb * 2 + wave : (gb - 32) * 12 + wave;
    if (FINE ? wave < 2 : rr < 256) redln_row<FINE ? 12 : 8>(ws, rW, mb * 256 + rr);
    signal_flag(ws, WS_FLN + (u32)mb * 64);
    if (mb == TLMB) TL((FINE ? 0 : 128) + gb, 21);
  }
  if (FINE) {
    if (gb < 64) {
      wait_flag(ws, WS_FLN + (u32)mb * 64, 128);
      acquire_bar();
    if (mb == TLMB) TL((FINE ? 0 : 128) + gb, 22);
      const FP p = getp();
      gtile<1, 3, 2, E_GLU>((const u16*)(ws + WS_HN), D_, p.wg, p.wu, F_, 10, mb * 256 + (gb & 3) * 64, (gb >> 2) * 96, rW, WS_GU, F_, nullptr, nullptr, smem);
      signal_flag(ws, WS_FGL + (u32)mb * 64);
    if (mb == TLMB) TL((FINE ? 0 : 128) + gb, 23);
    } else if (gb < 104) {
      wait_flag(ws, WS_FGL + (u32)mb * 64, 64);
      acquire_bar();
    if (mb == TLMB) TL((FINE ? 0 : 128) + gb, 24);
      const FP p = getp();
      gtile<1, 1, 4, E_DOWN>((const u16*)(ws + WS_GU), F_, p.wd, nullptr, D_, 24, mb * 256 + ((gb - 64) & 3) * 64, ((gb - 64) >> 2) * 64, rW, 0, D_, (const u16*)(ws + WS_H), p.out, smem);
      __syncthreads();
    if (mb == TLMB) TL((FINE ? 0 : 128) + gb, 25);
    }
  } else {
    if (gb >= 72 && gb < 88) {
      wait_flag(ws, WS_FLN + (u32)mb * 64, 22);
      acquire_bar();
    if (mb == TLMB) TL((FINE ? 0 : 128) + gb, 22);
      const FP p = getp();
      gtile<4, 3, 1, E_GLU>((const u16*)(ws + WS_HN), D_, p.wg, p.wu, F_, 10, mb * 256, (gb - 72) * 96, rW, WS_GU, F_, nullptr, nullptr, smem);
      signal_flag(ws, WS_FGL + (u32)mb * 64);
    if (mb == TLMB) TL((FINE ? 0 : 128) + gb, 23);
    } else if (gb >= 88) {
      wait_flag(ws, WS_FGL + (u32)mb * 64, 16);
      acquire_bar();
    if (mb == TLMB) TL((FINE ? 0 : 128) + gb, 24);
      const FP p = getp();
      gtile<1, 1, 4, E_DOWN>((const u16*)(ws + WS_GU), F_, p.wd, nullptr, D_, 24, mb * 256 + ((gb - 88) & 3) * 64, ((gb - 88) >> 2) * 64, rW, 0, D_, (const u16*)(ws + WS_H), p.out, smem);
      __syncthreads();
    if (mb == TLMB) TL((FINE ? 0 : 128) + gb, 25);
    }
  }
}

__device__ __forceinline__ void gemm_role(int gb, char* smem) {
  char* ws = getp().ws;
  rsrc_t rW = __builtin_amdgcn_make_buffer_rsrc(ws, 0, WS_END, 0x00020000);
  TL(128 + gb, 0);
  {
    int n = gb >> 1, h = n & 31;
    const FP p = getp();
    gtile<2, 3, 2, E_BF16>((const u16*)(ws + WS_XN), D_, n < 32 ? p.Wk : p.Wv, nullptr, HE_, 10, (gb & 1) * 128, h * E_, rW, n < 32 ? WS_KX : WS_VX, HE_, nullptr, nullptr, smem);
    signal_flag(ws, WS_FQKV + (u32)n * 64);
  }
#pragma unroll 1
  for (int a = gb + 64; a < 768; a += 128) {
    int mb = a / 96, n = a % 96; int mat = n >> 5, h = n & 31;
    const FP p = getp();
    const u16* W = mat == 0 ? p.Wk : (mat == 1 ? p.Wv : p.Wq);
    u32 coff = mat == 0 ? WS_KX : (mat == 1 ? WS_VX : WS_QX);
    gtile<4, 3, 1, E_BF16>((const u16*)(ws + WS_XN), D_, W, W, HE_, 10, mb * 256, h * E_, rW, coff, HE_, nullptr, nullptr, smem);
    signal_flag(ws, WS_FQKV + (u32)(mb * 96 + n) * 64);
    TL(128 + gb, 1 + a / 128);
  }
#pragma unroll 1
  for (int mb = 0; mb < 7; ++mb) {
    phase_b<false>(mb, gb, ws, rW, smem);
    TL(128 + gb, 8 + mb);
  }
}

constexpr int MST = 656, QST = 80, VST = 208, PST = 72;
constexpr int CL_MEM = 0;
constexpr int CL_Q   = 8 * MST * 2;
constexpr int KBUF   = 48 * QST * 2;
constexpr int VBUF   = 40 * VST * 2;
constexpr int CL_K   = CL_Q + 8 * QST * 2;
constexpr int CL_V   = CL_K + 2 * KBUF;
constexpr int CL_P   = CL_V + 2 * VBUF;
constexpr int CL_F   = CL_P + 8 * PST * 2;
constexpr int CL_END = CL_F + 16;
constexpr int CL_STG = CL_Q;

#define DLY_A2I 0
#define DLY_S 8
#define DLY_B 4
#define DLY_V 3
#define DLY_A1 0
#define DLY_A2 0
#define NPOLL 11
#define STAMP(i) do {} while (0)

__device__ __forceinline__ void chain_role(int cu, char* smem) {
  const FP p = getp();
  const int h = cu >> 2, j = cu & 3;
  const int tid = threadIdx.x, lane = tid & 63;
  const int wave = __builtin_amdgcn_readfirstlane(tid >> 6);
  const int fr = lane & 15, g = lane >> 4, q4 = fr >> 2, p4 = fr & 3;
  char* ws = p.ws;
  u32* ctl = (u32*)(ws + WS_CTL);
  rsrc_t rW = __builtin_amdgcn_make_buffer_rsrc(ws, 0, WS_END, 0x00020000);
  const u32 a2base0 = WS_GA2 + (u32)(cu % A2_REP) * A2_STRIDE;

  TL(cu, 0);
  bf16x8 Wf[20];
  bf16x8 Wof[6];
  {
#pragma unroll 1
    for (int rnd = 0; rnd < 2; ++rnd) {
      if (rnd) __syncthreads();
#pragma unroll 1
      for (int ii = wave; ii < 120; ii += 12) {
        int m2 = ii / 60, c = (ii % 60) * 64 + lane;
        const u16* sp;
        if (rnd == 1 && m2 == 1) { int e = c / 20, ch = c % 20; sp = p.Wo + (size_t)(h * E_ + e) * D_ + j * 160 + ch * 8; }
        else { const u16* W = rnd == 0 ? (m2 == 0 ? p.Wq : p.Wk) : p.Wv; int d = c / 6, ch = c % 6; sp = W + (size_t)d * HE_ + h * E_ + j * 48 + ch * 8; }
        __builtin_amdgcn_global_load_lds((const u32*)sp, (LAS u32*)(smem + ii * 1024), 16, 0, 0);
      }
      asm volatile("s_waitcnt vmcnt(0)" ::: "memory");
      __syncthreads();
      const int mat = wave / 3;
      if ((rnd == 0 && mat < 2) || (rnd == 1 && mat == 2)) {
        const u16* stg = (const u16*)(smem + (rnd == 0 ? mat : 0) * 61440);
        int e0 = (wave % 3) * 16;
#pragma unroll
        for (int ks = 0; ks < 20; ++ks) {
          int r0 = ks * 32 + g * 8 + q4;
          Wf[ks] = cat8(tr_read(stg + r0 * 48 + e0 + p4 * 4), tr_read(stg + (r0 + 4) * 48 + e0 + p4 * 4));
        }
      }
    }
    const u16* stg = (const u16*)(smem + 61440);
    const int wc = wave < 10 ? wave : 0;
#pragma unroll
    for (int t = 0; t < 6; ++t) {
      int r0 = t * 32 + g * 8 + q4;
      Wof[t] = cat8(tr_read(stg + r0 * 160 + wc * 16 + p4 * 4), tr_read(stg + (r0 + 4) * 160 + wc * 16 + p4 * 4));
    }
    __syncthreads();
  }
  for (int i = tid; i < (CL_END - CL_Q) / 4; i += NTH) ((u32*)(smem + CL_Q))[i] = 0u;
  __syncthreads();

  u16* memL = (u16*)(smem + CL_MEM);
  u16* qL = (u16*)(smem + CL_Q);
  u16* pL = (u16*)(smem + CL_P);
  volatile LAS u32* vflag = (volatile LAS u32*)(smem + CL_F);
  volatile LAS u32* pflag = vflag + 1;
  volatile LAS u32* rflag = vflag + 2;
  bool dead = false;

  auto xfetch = [&](int seg, int bf, int ii, int ln) {
    u32 off;
    if (ii < 5) { int c = ii * 64 + ln; int tok = c / 10, ch = c % 10;
      off = ch < 6 ? WS_KX + (u32)((seg * SEG_ + tok) * HE_ + h * E_ + j * 48 + ch * 8) * 2 : 2048u;
      __builtin_amdgcn_global_load_lds((const u32*)(ws + off), (LAS u32*)(smem + CL_K + bf * KBUF + 8 * QST * 2 + ii * 1024), 16, 0, 16);
    } else { int c = (ii - 5) * 64 + ln; int tok = c / 26, ch = c % 26; if (ch >= 24) ch = 0;
      off = WS_VX + (u32)((seg * SEG_ + tok) * HE_ + h * E_ + ch * 8) * 2;
      __builtin_amdgcn_global_load_lds((const u32*)(ws + off), (LAS u32*)(smem + CL_V + bf * VBUF + 8 * VST * 2 + (ii - 5) * 1024), 16, 0, 16);
    }
  };
  auto wait_kv = [&](int mb) {
    u32* fk = (u32*)(ws + WS_FQKV + (u32)(mb * 96 + h) * 64); u32* fv = (u32*)(ws + WS_FQKV + (u32)(mb * 96 + 32 + h) * 64);
    u32 spins = 0;
    for (;;) {
      u32 a = __hip_atomic_load(fk, RLX_AGENT), b = __hip_atomic_load(fv, RLX_AGENT);
      const u32 need = mb == 0 ? 2u : 1u;
      if (__builtin_amdgcn_readfirstlane(a) >= need && __builtin_amdgcn_readfirstlane(b) >= need) break;
      __builtin_amdgcn_s_sleep(4);
      if (dead || spin_fail(spins, ctl)) { dead = true; break; }
    }
    asm volatile("" ::: "memory");
  };
  TL(cu, 1);
  wait_kv(0);
  TL(cu, 2);
  for (int ii = wave; ii < 18; ii += 12) xfetch(0, 0, ii, lane);
  asm volatile("s_waitcnt vmcnt(0)" ::: "memory");
  __syncthreads();
  u32 vwx[4];
  auto calc_vwx = [&](int bf, int fr_, int g_) {
    const u16* vb = (const u16*)(smem + CL_V + bf * VBUF) + (8 + fr_) * VST + g_ * 8;
    f32x4 a0 = {0.f, 0.f, 0.f, 0.f}, a1 = {0.f, 0.f, 0.f, 0.f};
#pragma unroll
    for (int t = 0; t < 6; ++t) {
      a0 = mfma16(*(const bf16x8*)(vb + t * 32), Wof[t], a0);
      a1 = mfma16(*(const bf16x8*)(vb + 16 * VST + t * 32), Wof[t], a1);
    }
    vwx[0] = pack2(a0[0], a0[1]); vwx[1] = pack2(a0[2], a0[3]); vwx[2] = pack2(a1[0], a1[1]); vwx[3] = pack2(a1[2], a1[3]);
  };
  calc_vwx(0, fr, g);

#pragma unroll 1
  for (int s = 0; s < NSEG_; ++s) {
    int tz = threadIdx.x; asm volatile("" : "+v"(tz));
    const int tid = tz, lane = tz & 63, fr = lane & 15, g = lane >> 4;
    const u32 ep = (u32)s + 1u;
    const u32 par = ((u32)(s / RNG) + 1u) & 1u;
    const u32 WS_GB = WS_GB0 + (u32)(s % RNG) * GB_SZ, WS_GA1 = WS_GA10 + (u32)(s % RNG) * A1_SZ;
    const u32 a2w = (u32)(s % RNG) * A2_SZ, a2base = a2base0 + (u32)((s + RNG - 1) % RNG) * A2_SZ;
    const int buf = s & 1;
    u16* kL = (u16*)(smem + CL_K + buf * KBUF);
    u16* vL = (u16*)(smem + CL_V + buf * VBUF);
    STAMP(0);
    if (s == 0) {
      for (int c = tid; c < 8 * 80; c += NTH) { int r = c / 80, ch = c % 80; *(u32x4*)(memL + r * MST + ch * 8) = *(const u32x4*)(getp().mem0 + r * D_ + ch * 8); }
    } else if (wave < 10) {
      const u32 epm = (u32)s;
      u32x4 a0, a1; u32 spins = 0;
      for (int i = 0; i < DLY_A2I; ++i) __builtin_amdgcn_s_sleep(4);
      for (;;) {
        asm volatile("" ::: "memory");
        a0 = ld_sc1_16(rW, a2base + tid * 16); a1 = ld_sc1_16(rW, a2base + (tid + 640) * 16);
        bool ok = a0[1] == epm && a0[3] == epm && a1[1] == epm && a1[3] == epm;
        if (__all(ok)) break;
        for (int i = 0; i < DLY_A2; ++i) __builtin_amdgcn_s_sleep(2);
        if (dead || spin_fail(spins, ctl)) { dead = true; break; }
      }
      { int u = tid; int row = ((u >> 4) & 1) * 4 + ((u >> 2) & 3), col = (u >> 5) * 16 + (u & 3) * 4; *(u32x2*)(memL + row * MST + col) = (u32x2){a0[0], a0[2]}; }
      { int u = tid + 640; int row = ((u >> 4) & 1) * 4 + ((u >> 2) & 3), col = (u >> 5) * 16 + (u & 3) * 4; *(u32x2*)(memL + row * MST + col) = (u32x2){a1[0], a1[2]}; }
    }
    STAMP(1);
    lbar();
    STAMP(2);
    if (wave < 9) {
      f32x4 acc = {0.f, 0.f, 0.f, 0.f};
      const u16* mb = memL + (fr & 7) * MST + g * 8;
      bf16x8 b0[4], b1[4];
#pragma unroll
      for (int i = 0; i < 4; ++i) b0[i] = *(const bf16x8*)(mb + i * 32);
#pragma unroll
      for (int gq = 0; gq < 5; ++gq) {
        if (gq & 1) {
          if (gq < 4) {
#pragma unroll
            for (int i = 0; i < 4; ++i) b0[i] = *(const bf16x8*)(mb + ((gq + 1) * 4 + i) * 32);
          }
#pragma unroll
          for (int i = 0; i < 4; ++i) acc = mfma16(Wf[gq * 4 + i], b1[i], acc);
        } else {
          if (gq < 4) {
#pragma unroll
            for (int i = 0; i < 4; ++i) b1[i] = *(const bf16x8*)(mb + ((gq + 1) * 4 + i) * 32);
          }
#pragma unroll
          for (int i = 0; i < 4; ++i) acc = mfma16(Wf[gq * 4 + i], b0[i], acc);
        }
        __builtin_amdgcn_sched_barrier(0);
      }
      u32x2 w = {pack2(acc[0], acc[1]), pack2(acc[2], acc[3])};
      int e = (wave % 3) * 16 + g * 4;
      if (fr < 8) {
        if (wave < 3) *(u32x2*)(qL + fr * QST + e) = w;
        else if (wave < 6) {
          *(u32x2*)(kL + fr * QST + e) = w;
          st_sc1_8(rW, WS_KM + (u32)((s * M_ + fr) * HE_ + h * E_ + j * 48 + e) * 2, w);
        } else {
          st_sc1_16(rW, WS_GB + (u32)(cu * 192 + 96 + (wave - 6) * 32 + fr * 4 + g) * 16, (u32x4){w[0], ep, w[1], ep});
          st_sc1_8(rW, WS_VM + (u32)((s * M_ + fr) * HE_ + h * E_ + j * 48 + e) * 2, w);
        }
      }
    }
    if (s == NSEG_ - 1) break;
    lbar();
    STAMP(3);
    if (wave == 10) {
      if (((s + 1) & 7) == 0) wait_kv((s + 1) >> 3);
#pragma unroll 1
      for (int ii = 0; ii < 18; ++ii) xfetch(s + 1, buf ^ 1, ii, lane);
    }

    if (wave < 3) {
      f32x4 acc = {0.f, 0.f, 0.f, 0.f};
#pragma unroll
      for (int kk = 0; kk < 2; ++kk) {
        bf16x8 a = *(const bf16x8*)(kL + (wave * 16 + fr) * QST + kk * 32 + g * 8);
        bf16x8 b = *(const bf16x8*)(qL + (fr & 7) * QST + kk * 32 + g * 8);
        acc = mfma16(a, b, acc);
      }
      if (fr < 8)
        st_sc1_16(rW, WS_GB + (u32)(cu * 192 + wave * 32 + fr * 4 + g) * 16,
                  (u32x4){(__float_as_uint(acc[0]) & ~1u) | par, (__float_as_uint(acc[1]) & ~1u) | par,
                          (__float_as_uint(acc[2]) & ~1u) | par, (__float_as_uint(acc[3]) & ~1u) | par});
    }
    STAMP(4);
    if (wave >= NPOLL) {
      const int r = lane & 7, q = lane >> 3;
      bool got = true;
      for (int i = 0; i < DLY_B; ++i) __builtin_amdgcn_s_sleep(4);
      const u32 o0 = WS_GB + (u32)(h * 4 * 192 + (q >> 2) * 32 + r * 4 + (q & 3)) * 16;
      const u32 o1 = WS_GB + (u32)(h * 4 * 192 + 64 + r * 4 + (q & 1)) * 16;
      u32x4 a[4], b[4]; u32 spins = 0;
      for (;;) {
        asm volatile("" ::: "memory");
        u32 aa = 0xffffffffu, oo = 0u;
#pragma unroll
        for (int i = 0; i < 4; ++i) {
          a[i] = __builtin_amdgcn_raw_buffer_load_b128(rW, o0, i * 3072, 16);
          b[i] = __builtin_amdgcn_raw_buffer_load_b128(rW, o1, i * 3072, 16);
        }
#pragma unroll
        for (int i = 0; i < 4; ++i) {
          aa &= a[i][0] & a[i][1] & a[i][2] & a[i][3] & b[i][0] & b[i][1] & b[i][2] & b[i][3];
          oo |= a[i][0] | a[i][1] | a[i][2] | a[i][3] | b[i][0] | b[i][1] | b[i][2] | b[i][3];
        }
        bool ok = (aa & 1u) == par && (oo & 1u) == par;
        if (__all(ok)) break;
        if (*pflag == ep) { got = false; break; }
        if (dead || spin_fail(spins, ctl)) { dead = true; break; }
      }
      STAMP(5);
      if (got) {
      const float scale = 0.07216878364870322f;
      float sc[8];
#pragma unroll
      for (int k = 0; k < 4; ++k) {
        float x = (__uint_as_float(a[0][k]) + __uint_as_float(a[1][k])) + (__uint_as_float(a[2][k]) + __uint_as_float(a[3][k]));
        float y = (__uint_as_float(b[0][k]) + __uint_as_float(b[1][k])) + (__uint_as_float(b[2][k]) + __uint_as_float(b[3][k]));
        sc[k] = rbf(rbf(x) * scale); sc[4 + k] = rbf(rbf(y) * scale);
      }
      float mx = fmaxf(fmaxf(fmaxf(sc[0], sc[1]), fmaxf(sc[2], sc[3])), fmaxf(fmaxf(sc[4], sc[5]), fmaxf(sc[6], sc[7])));
      mx = fmaxf(mx, __uint_as_float(__builtin_amdgcn_update_dpp(0, __float_as_uint(mx), 0x128, 0xf, 0xf, false)));
      mx = red16_max(mx); mx = red32_max(mx);
      float sum = 0.f, sum2 = 0.f;
#pragma unroll
      for (int k = 0; k < 4; ++k) { sc[k] = __expf(sc[k] - mx); sum += sc[k]; sc[4 + k] = __expf(sc[4 + k] - mx); sum2 += sc[4 + k]; }
      if (q < 2) sum += sum2;
      sum += __uint_as_float(__builtin_amdgcn_update_dpp(0, __float_as_uint(sum), 0x128, 0xf, 0xf, false));
      sum = red16_add(sum); sum = red32_add(sum);
      float inv = __builtin_amdgcn_rcpf(sum);
      *(u32x2*)(pL + r * PST + q * 4) = (u32x2){pack2(sc[0] * inv, sc[1] * inv), pack2(sc[2] * inv, sc[3] * inv)};
      if (q < 2) *(u32x2*)(pL + r * PST + 32 + q * 4) = (u32x2){pack2(sc[4] * inv, sc[5] * inv), pack2(sc[6] * inv, sc[7] * inv)};
      asm volatile("s_waitcnt lgkmcnt(0)" ::: "memory");
      if (lane == 0) *pflag = ep;
      }
      STAMP(6);
    } else if (wave == 9) {
      u32x4 v[6]; u32 spins = 0;
      for (int i = 0; i < DLY_V; ++i) __builtin_amdgcn_s_sleep(4);
      for (;;) {
        asm volatile("" ::: "memory");
        bool ok = true;
#pragma unroll
        for (int i = 0; i < 6; ++i) { int id = lane + 64 * i; int src = id / 96, u = id % 96; v[i] = ld_sc1_16(rW, WS_GB + (u32)((h * 4 + src) * 192 + 96 + u) * 16); }
#pragma unroll
        for (int i = 0; i < 6; ++i) ok = ok && v[i][1] == ep && v[i][3] == ep;
        if (__all(ok)) break;
        if (dead || spin_fail(spins, ctl)) { dead = true; break; }
      }
#pragma unroll
      for (int i = 0; i < 6; ++i) {
        int id = lane + 64 * i; int src = id / 96, u = id % 96; int vt = u >> 5, rr = (u >> 2) & 7, gg = u & 3;
        *(u32x2*)(vL + rr * VST + src * 48 + vt * 16 + gg * 4) = (u32x2){v[i][0], v[i][2]};
      }
      asm volatile("s_waitcnt lgkmcnt(0)" ::: "memory");
      if (lane == 0) *vflag = ep;
    }
    if (wave < 10) {
      while (*vflag != ep) { if (dead) break; }
      asm volatile("" ::: "memory");
      f32x4 am = {0.f, 0.f, 0.f, 0.f};
      const u16* vb = vL + (fr & 7) * VST + g * 8;
#pragma unroll
      for (int t = 0; t < 6; ++t) am = mfma16(*(const bf16x8*)(vb + t * 32), Wof[t], am);
      u32x4 a0u = {vwx[0], vwx[1], vwx[2], vwx[3]};
      u32x4 a1u = {g < 2 ? pack2(am[0], am[1]) : 0u, g < 2 ? pack2(am[2], am[3]) : 0u, 0u, 0u};
      while (*pflag != ep) { if (dead) break; }
      asm volatile("s_waitcnt vmcnt(0)" ::: "memory");
      const u16* pr = pL + (fr & 7) * PST;
      u32x2 x0 = *(const u32x2*)(pr + 8 + g * 4), x1 = *(const u32x2*)(pr + 24 + g * 4), x2 = *(const u32x2*)(pr + (g & 1) * 4);
      u32x4 b0u = {x0[0], x0[1], x1[0], x1[1]};
      u32x4 b1u = {g < 2 ? x2[0] : 0u, g < 2 ? x2[1] : 0u, 0u, 0u};
      f32x4 acc = {0.f, 0.f, 0.f, 0.f};
      acc = mfma16(__builtin_bit_cast(bf16x8, a0u), __builtin_bit_cast(bf16x8, b0u), acc);
      acc = mfma16(__builtin_bit_cast(bf16x8, a1u), __builtin_bit_cast(bf16x8, b1u), acc);
      if (fr < 8) {
        u32 o = WS_GA1 + (u32)(((j * 10 + wave) * 32 + h) * 32 + fr * 4 + g) * 16;
        st_sc1_16(rW, o, (u32x4){(__float_as_uint(acc[0]) & ~1u) | par, (__float_as_uint(acc[1]) & ~1u) | par,
                                 (__float_as_uint(acc[2]) & ~1u) | par, (__float_as_uint(acc[3]) & ~1u) | par});
      }
    }
    STAMP(8);
    if (wave == 10) asm volatile("s_waitcnt vmcnt(0)" ::: "memory");
    lbar();
    if ((s & 7) == 7) TL(cu, 3 + (s >> 3));
    if ((s & 7) == 7 && tid == 0) __hip_atomic_store((u32*)(ws + WS_FCH + (u32)cu * 64), (u32)(s >> 3) + 1u, RLX_AGENT);
    if (wave < 10) calc_vwx(buf ^ 1, fr, g);
    if (wave >= NPOLL) {
      if (cu < 80) {
        const int ch = lane & 15, hq = lane >> 4;
        bool got = true;
        for (int i = 0; i < DLY_A1; ++i) __builtin_amdgcn_s_sleep(4);
        const u32 rbase = WS_GA1 + (u32)(((cu >> 1) * 32 + hq * 8) * 32 + (cu & 1) * 16 + ch) * 16;
        u32x4 a[8]; u32 spins = 0;
        for (;;) {
          asm volatile("" ::: "memory");
          u32 aa = 0xffffffffu, oo = 0u;
#pragma unroll
          for (int i = 0; i < 8; ++i) a[i] = __builtin_amdgcn_raw_buffer_load_b128(rW, rbase, i * 512, 16);
#pragma unroll
          for (int i = 0; i < 8; ++i) { aa &= a[i][0] & a[i][1] & a[i][2] & a[i][3]; oo |= a[i][0] | a[i][1] | a[i][2] | a[i][3]; }
          bool ok = (aa & 1u) == par && (oo & 1u) == par;
          if (__all(ok)) break;
          if (*rflag == ep) { got = false; break; }
          if (dead || spin_fail(spins, ctl)) { dead = true; break; }
        }
        STAMP(9);
        if (got) {
        float t[4];
#pragma unroll
        for (int k = 0; k < 4; ++k) {
          float x = ((__uint_as_float(a[0][k]) + __uint_as_float(a[1][k])) + (__uint_as_float(a[2][k]) + __uint_as_float(a[3][k])))
                  + ((__uint_as_float(a[4][k]) + __uint_as_float(a[5][k])) + (__uint_as_float(a[6][k]) + __uint_as_float(a[7][k])));
          x = red16_add(x); x = red32_add(x); t[k] = x;
        }
        u32 w0 = pack2(t[0], t[1]), w1 = pack2(t[2], t[3]);
#pragma unroll
        for (int it = 0; it < A2_REP / 4; ++it) st_sc1_16(rW, a2w + WS_GA2 + (u32)(cu * 16 + ch) * 16 + (u32)(hq + 4 * it) * A2_STRIDE, (u32x4){w0, ep, w1, ep});
        if (lane == 0) *rflag = ep;
        }
        STAMP(10);
      }
      else if (wave == 11) {
        u32 spins = 0;
        for (int i = 0; i < DLY_S; ++i) __builtin_amdgcn_s_sleep(4);
        for (;;) {
          asm volatile("" ::: "memory");
          u32x4 a = ld_sc1_16(rW, a2base0 + a2w + (u32)(cu - 80) * 256);
          if (__all(a[1] == ep)) break;
          if (dead || spin_fail(spins, ctl)) { dead = true; break; }
        }
      }
    }
    lbar();
  }
  TL(cu, 10);
  asm volatile("s_waitcnt vmcnt(0)" ::: "memory");
  __syncthreads();
  if (tid == 0) __hip_atomic_store((u32*)(ws + WS_FCH + (u32)cu * 64), 8u, RLX_AGENT);
  phase_b<true>(7, cu, ws, rW, smem);
  TL(cu, 11);
}

__global__ __launch_bounds__(NTH, 3) void k_main(FP p) {
  extern __shared__ __attribute__((aligned(16))) char smem[];
  const int b = blockIdx.x;
  const int id = (b >> 4) * 8 + (b & 7);
  if ((b >> 3) & 1) gemm_role(id, smem);
  else chain_role(id, smem);
}

extern "C" void kernel_launch(void* const* d_in, const int* in_sizes, int n_in,
                              void* d_out, int out_size, void* d_ws, size_t ws_size,
                              hipStream_t stream) {
  char* ws = (char*)d_ws;
  static bool attr_done = false;
  if (!attr_done) {
    (void)hipFuncSetAttribute((const void*)k_main, hipFuncAttributeMaxDynamicSharedMemorySize, LDS_BYTES);
    attr_done = true;
  }
  k_ln<<<S_ / 4, 256, 0, stream>>>((const u16*)d_in[0], (u16*)(ws + WS_XN), (u32x4*)ws);
  FP p{(const u16*)d_in[0], (const u16*)d_in[1], (const u16*)d_in[2], (const u16*)d_in[3], (const u16*)d_in[4], (const u16*)d_in[5],
       (const u16*)d_in[8], (const u16*)d_in[9], (const u16*)d_in[10], (u16*)d_out, ws};
  k_main<<<256, NTH, LDS_BYTES, stream>>>(p);
}
```

```cpp
#include <hip/hip_runtime.h>
#include <hip/hip_bf16.h>
#include <stdint.h>

typedef unsigned short u16;
typedef unsigned int u32;
typedef unsigned long long u64;
typedef short bf16x8 __attribute__((ext_vector_type(8)));
typedef short bf16x4 __attribute__((ext_vector_type(4)));
typedef float f32x4 __attribute__((ext_vector_type(4)));
typedef u32 u32x4 __attribute__((ext_vector_type(4)));
typedef u32 u32x2 __attribute__((ext_vector_type(2)));
#define LAS __attribute__((address_space(3)))
#define RLX_AGENT __ATOMIC_RELAXED, __HIP_MEMORY_SCOPE_AGENT

constexpr int S_ = 2048, D_ = 640, H_ = 32, E_ = 192, HE_ = H_ * E_;
constexpr int M_ = 8, SEG_ = 32, NSEG_ = 64, F_ = 1536;

constexpr u32 WS_CTL   = 0;
constexpr u32 WS_FQKV  = 4096;
constexpr u32 WS_FCH   = WS_FQKV + 8 * 96 * 64;
constexpr u32 WS_FAT   = WS_FCH + 128 * 64;
constexpr u32 WS_FOP   = WS_FAT + 64 * 64;
constexpr u32 WS_FLN   = WS_FOP + 8 * 64;
constexpr u32 WS_FGL   = WS_FLN + 8 * 64;
constexpr u32 WS_GA2   = (WS_FGL + 8 * 64 + 4095) / 4096 * 4096;
constexpr int A2R = 8;
constexpr int A2_REP = A2R, A2_STRIDE = 20736;
constexpr int RNG = 8;
constexpr u32 A2_SZ = A2_REP * A2_STRIDE, GB_SZ = 128 * 3072, A1_SZ = 40 * 32 * 512;
constexpr u32 WS_GB0   = WS_GA2 + RNG * A2_SZ;
constexpr u32 WS_GA10  = WS_GB0 + RNG * GB_SZ;
constexpr u32 WS_ZEND  = WS_GA10 + RNG * A1_SZ;
constexpr u32 WS_XN    = (WS_ZEND + 4095) / 4096 * 4096;
constexpr u32 WS_KX    = WS_XN + S_ * D_ * 2;
constexpr u32 WS_VX    = WS_KX + S_ * HE_ * 2;
constexpr u32 WS_QX    = WS_VX + S_ * HE_ * 2;
constexpr u32 WS_KM    = WS_QX + S_ * HE_ * 2;
constexpr u32 WS_VM    = WS_KM + NSEG_ * M_ * HE_ * 2;
constexpr u32 WS_OX    = WS_VM + NSEG_ * M_ * HE_ * 2;
constexpr u32 WS_H     = WS_OX + S_ * HE_ * 2;
constexpr u32 WS_HN    = WS_H + S_ * D_ * 2;
constexpr u32 WS_GU    = WS_HN + S_ * D_ * 2;
constexpr u32 WS_SLAB  = WS_GU + S_ * F_ * 2;
constexpr int NSLAB = 12;
constexpr u32 WS_END   = WS_SLAB + (u32)NSLAB * S_ * D_ * 4;

__device__ __forceinline__ float bf2f(u16 v) { return __uint_as_float(((u32)v) << 16); }
__device__ __forceinline__ u16 f2bf(float f) {
  __hip_bfloat16 b = __float2bfloat16(f);
  return *reinterpret_cast<u16*>(&b);
}
__device__ __forceinline__ float rbf(float f) { return bf2f(f2bf(f)); }
__device__ __forceinline__ u32 pack2(float a, float b) { return (u32)f2bf(a) | ((u32)f2bf(b) << 16); }
__device__ __forceinline__ f32x4 mfma16(bf16x8 a, bf16x8 b, f32x4 c) {
  return __builtin_amdgcn_mfma_f32_16x16x32_bf16(a, b, c, 0, 0, 0);
}
__device__ __forceinline__ bf16x4 tr_read(const void* p) {
  return __builtin_amdgcn_ds_read_tr16_b64_v4i16((LAS bf16x4*)p);
}
__device__ __forceinline__ bf16x8 cat8(bf16x4 a, bf16x4 b) {
  bf16x8 r; r[0]=a[0]; r[1]=a[1]; r[2]=a[2]; r[3]=a[3]; r[4]=b[0]; r[5]=b[1]; r[6]=b[2]; r[7]=b[3]; return r;
}
__device__ __forceinline__ void lbar() { asm volatile("s_waitcnt lgkmcnt(0)\n\ts_barrier" ::: "memory"); }
typedef __amdgpu_buffer_rsrc_t rsrc_t;
__device__ __forceinline__ u32x4 ld_sc1_16(rsrc_t r, u32 off) { return __builtin_amdgcn_raw_buffer_load_b128(r, off, 0, 16); }
__device__ __forceinline__ void st_sc1_16(rsrc_t r, u32 off, u32x4 v) { __builtin_amdgcn_raw_buffer_store_b128(v, r, off, 0, 16); }
__device__ __forceinline__ void st_sc1_8(rsrc_t r, u32 off, u32x2 v) { __builtin_amdgcn_raw_buffer_store_b64(v, r, off, 0, 16); }
__device__ __forceinline__ float red16_max(float x) {
  auto r = __builtin_amdgcn_permlane16_swap(__float_as_uint(x), __float_as_uint(x), false, false);
  return fmaxf(__uint_as_float(r[0]), __uint_as_float(r[1]));
}
__device__ __forceinline__ float red32_max(float x) {
  auto r = __builtin_amdgcn_permlane32_swap(__float_as_uint(x), __float_as_uint(x), false, false);
  return fmaxf(__uint_as_float(r[0]), __uint_as_float(r[1]));
}
__device__ __forceinline__ float red16_add(float x) {
  auto r = __builtin_amdgcn_permlane16_swap(__float_as_uint(x), __float_as_uint(x), false, false);
  return __uint_as_float(r[0]) + __uint_as_float(r[1]);
}
__device__ __forceinline__ float red32_add(float x) {
  auto r = __builtin_amdgcn_permlane32_swap(__float_as_uint(x), __float_as_uint(x), false, false);
  return __uint_as_float(r[0]) + __uint_as_float(r[1]);
}

constexpr u32 SPIN_CAP = 1u << 18;
__device__ __forceinline__ bool spin_fail(u32& spins, u32* ctl) {
  ++spins;
  if (spins > SPIN_CAP) { __hip_atomic_store(ctl, 1u, RLX_AGENT); return true; }
  return (spins & 255u) == 0u && __builtin_amdgcn_readfirstlane(__hip_atomic_load(ctl, RLX_AGENT)) != 0u;
}

struct FP {
  const u16* x; const u16* mem0; const u16* Wq; const u16* Wk; const u16* Wv; const u16* Wo;
  const u16* wg; const u16* wu; const u16* wd; u16* out; char* ws;
};
typedef const __attribute__((address_space(4))) FP* kargp_t;
__device__ __forceinline__ FP getp() {
#if defined(__HIP_DEVICE_COMPILE__)
  kargp_t kp = (kargp_t)__builtin_amdgcn_kernarg_segment_ptr(); asm volatile("" : "+s"(kp)); return *kp;
#else
  return FP{};
#endif
}
constexpr int NTH = 768;
constexpr int LDS_BYTES = 153600;

__device__ __forceinline__ void ln_row(const float (&v)[10], u32 (&o)[5]) {
  float sum = 0.f;
#pragma unroll
  for (int i = 0; i < 10; ++i) sum += v[i];
#pragma unroll
  for (int m = 32; m >= 1; m >>= 1) sum += __shfl_xor(sum, m);
  float mean = sum * (1.0f / D_);
  float sq = 0.f;
#pragma unroll
  for (int i = 0; i < 10; ++i) { float d = v[i] - mean; sq += d * d; }
#pragma unroll
  for (int m = 32; m >= 1; m >>= 1) sq += __shfl_xor(sq, m);
  float var = sq * (1.0f / D_);
  float mb = rbf(mean), vb = rbf(var);
  float den = rbf(sqrtf(rbf(vb + 1e-5f)));
#pragma unroll
  for (int i = 0; i < 5; ++i) o[i] = pack2(rbf(v[2 * i] - mb) / den, rbf(v[2 * i + 1] - mb) / den);
}
__global__ __launch_bounds__(256) void k_ln(const u16* __restrict__ x, u16* __restrict__ y, u32x4* __restrict__ zero) {
  for (u32 i = blockIdx.x * 256 + threadIdx.x; i < WS_ZEND / 16; i += (S_ / 4) * 256) zero[i] = (u32x4){0u, 0u, 0u, 0u};
  int row = blockIdx.x * 4 + (threadIdx.x >> 6);
  int lane = threadIdx.x & 63;
  const u32* xr = (const u32*)(x + (size_t)row * D_);
  float v[10];
#pragma unroll
  for (int i = 0; i < 5; ++i) { u32 w = xr[i * 64 + lane]; v[2 * i] = bf2f((u16)(w & 0xffff)); v[2 * i + 1] = bf2f((u16)(w >> 16)); }
  u32 o[5]; ln_row(v, o);
  u32* yr = (u32*)(y + (size_t)row * D_);
#pragma unroll
  for (int i = 0; i < 5; ++i) yr[i * 64 + lane] = o[i];
}

__device__ __forceinline__ void wait_flag(char* ws, u32 foff, u32 target) {
  if (threadIdx.x == 0) {
    u32* f = (u32*)(ws + foff); u32* ctl = (u32*)(ws + WS_CTL); u32 spins = 0;
    while (__hip_atomic_load(f, RLX_AGENT) < target) {
      __builtin_amdgcn_s_sleep(8);
      if (++spins > (1u << 20)) { __hip_atomic_store(ctl, 1u, RLX_AGENT); break; }
      if ((spins & 63u) == 0u && __hip_atomic_load(ctl, RLX_AGENT) != 0u) break;
    }
  }
}
__device__ __forceinline__ void acquire_bar() {
  if (threadIdx.x == 0) { __builtin_amdgcn_fence(__ATOMIC_ACQUIRE, "agent"); asm volatile("s_waitcnt vmcnt(0)" ::: "memory"); }
  __syncthreads();
}
__device__ __forceinline__ void signal_flag(char* ws, u32 foff) {
  asm volatile("s_waitcnt vmcnt(0)" ::: "memory");
  __syncthreads();
  if (threadIdx.x == 0) __hip_atomic_fetch_add((u32*)(ws + foff), 1u, RLX_AGENT);
}

template <int NWN> __device__ __forceinline__ int bswz(int row) {
  if (NWN == 3) return (((row >> 1) & 1) | (((row >> 3) & 1) << 1)) << 1;
  else if (NWN == 2) return ((row & 3) << 2) | ((row >> 2) & 3);
  else return (((row >> 1) & 1) | (((row >> 3) & 1) << 1)) << 1;
}
enum { E_BF16 = 0, E_SLAB = 1, E_GLU = 2, E_DOWN = 3 };
template <int NWM, int NWN, int KG, int EPI>
__device__ __forceinline__ void gtile(const u16* __restrict__ A, int lda, const u16* __restrict__ B0, const u16* __restrict__ B1, int ldb,
                                      int nk, int m0, int nb, rsrc_t rW, u32 coff, int ldc, const u16* resid, u16* outp, char* smem) {
  constexpr int BM = NWM * 64, BN = NWN * 64, BROW = BN * 2, NA = BM / 16, NI = NA + BN / 16, NT = NI * KG, ST_BYTES = NT * 1024;
  constexpr int NST = (143360 / ST_BYTES) < 16 ? (143360 / ST_BYTES) : 16, PD = NST - 1;
  constexpr int NPW = (NT + 11) / 12;
  constexpr int NW = NWM * NWN;
  int tz = threadIdx.x; asm volatile("" : "+v"(tz));
  const int tid = tz, lane = tid & 63, wave = __builtin_amdgcn_readfirstlane(tid >> 6);
  const int kg = wave / NW, wv = wave % NW, wm = wv % NWM, wn = wv / NWM;
  const int fr = lane & 15, g = lane >> 4;
  const u16* src[NPW]; u32 sstep[NPW];
#pragma unroll
  for (int i = 0; i < NPW; ++i) {
    int it = wave + 12 * i; if (it >= NT) it = 0;
    int sub = it / NI, ii = it % NI;
    if (ii < NA) { int b = ii * 1024 + lane * 16; int row = b >> 6, pos = (b >> 4) & 3, ch = pos ^ ((4 - (row >> 2)) & 3);
      src[i] = A + (size_t)(m0 + row) * lda + ch * 8 + sub * 32; sstep[i] = 32 * KG; }
    else { int b = (ii - NA) * 1024 + lane * 16; int row = b / BROW, pos = (b % BROW) >> 4;
      int ch = pos ^ bswz<NWN>(row);
      if (EPI == E_GLU) { int w2 = ch >> 3, within = ch & 7; const u16* base = (within >> 2) ? B1 : B0;
        src[i] = base + (size_t)(row + sub * 32) * ldb + nb + w2 * 32 + (within & 3) * 8; }
      else src[i] = B0 + (size_t)(row + sub * 32) * ldb + nb + ch * 8;
      sstep[i] = 32u * KG * (u32)ldb; }
  }
  const int npw = (wave + 12 * (NPW - 1)) < NT ? NPW : NPW - 1;
  auto stage = [&](int kt, int buf) {
    char* sb = smem + buf * ST_BYTES;
#pragma unroll
    for (int i = 0; i < NPW; ++i)
      if (i < npw) __builtin_amdgcn_global_load_lds((const u32*)(src[i] + (size_t)kt * sstep[i]), (LAS u32*)(sb + (wave + 12 * i) * 1024), 16, 0, 0);
  };
  f32x4 acc[4][4];
#pragma unroll
  for (int a = 0; a < 4; ++a)
#pragma unroll
    for (int b = 0; b < 4; ++b) acc[a][b] = (f32x4){0.f, 0.f, 0.f, 0.f};
  const bool compk = wave < NW * KG;
  const bool comp = wave < NW;
  const int nst = nk * 2 / KG;
#pragma unroll 1
  for (int i = 0; i < PD; ++i) if (i < nst) stage(i, i);
  int cb = 0, pb = PD;
  const int q = fr >> 2, pp = fr & 3;
  const int r0 = g * 8 + q, r1 = r0 + 4;
  const int x0 = bswz<NWN>(r0), x1 = bswz<NWN>(r1);
  const int aoff = kg * NI * 1024 + (wm * 64 + fr) * 64 + ((g ^ ((4 - (fr >> 2)) & 3)) << 4);
#pragma unroll 1
  for (int kt = 0; kt < nst; ++kt) {
    if (kt + PD <= nst) {
      if (npw == NPW) asm volatile("s_waitcnt vmcnt(%0)" :: "i"(NPW * (PD - 1)) : "memory");
      else asm volatile("s_waitcnt vmcnt(%0)" :: "i"((NPW - 1) * (PD - 1)) : "memory");
    }
    else asm volatile("s_waitcnt vmcnt(0)" ::: "memory");
    lbar();
    if (kt + PD < nst) stage(kt + PD, pb);
    if (compk) {
      const char* sa = smem + cb * ST_BYTES; const char* sb = sa + (kg * NI + NA) * 1024;
      bf16x8 af[4], bfr[4];
#pragma unroll
      for (int a = 0; a < 4; ++a) af[a] = *(const bf16x8*)(sa + aoff + a * 1024);
#pragma unroll
      for (int b = 0; b < 4; ++b) {
        int ch = wn * 8 + b * 2 + (pp >> 1);
        bfr[b] = cat8(tr_read(sb + r0 * BROW + ((ch ^ x0) << 4) + 8 * (pp & 1)), tr_read(sb + r1 * BROW + ((ch ^ x1) << 4) + 8 * (pp & 1)));
      }
#pragma unroll
      for (int a = 0; a < 4; ++a)
#pragma unroll
        for (int b = 0; b < 4; ++b) acc[a][b] = mfma16(bfr[b], af[a], acc[a][b]);
    }
    cb = cb == NST - 1 ? 0 : cb + 1; pb = pb == NST - 1 ? 0 : pb + 1;
  }
  if (KG > 1) {
    lbar();
    if (compk && kg > 0) {
      f32x4* d = (f32x4*)(smem + (wave - NW) * 16384) + lane;
#pragma unroll
      for (int a = 0; a < 4; ++a)
#pragma unroll
        for (int b = 0; b < 4; ++b) d[(a * 4 + b) * 64] = acc[a][b];
    }
    lbar();
    if (comp) {
#pragma unroll
      for (int k2 = 1; k2 < KG; ++k2) {
        const f32x4* d = (const f32x4*)(smem + ((k2 - 1) * NW + wv) * 16384) + lane;
#pragma unroll
        for (int a = 0; a < 4; ++a)
#pragma unroll
          for (int b = 0; b < 4; ++b) acc[a][b] += d[(a * 4 + b) * 64];
      }
    }
  }
  if (EPI == E_DOWN) {
    if (comp) {
#pragma unroll
      for (int a = 0; a < 4; ++a) {
        const int m = m0 + wm * 64 + a * 16 + fr;
#pragma unroll
        for (int b = 0; b < 4; ++b) {
          int n = nb + wn * 64 + b * 16 + g * 4;
          u32x2 rv = *(const u32x2*)(resid + (size_t)m * ldc + n);
          float o0 = rbf(acc[a][b][0]) + bf2f((u16)(rv[0] & 0xffff)), o1 = rbf(acc[a][b][1]) + bf2f((u16)(rv[0] >> 16));
          float o2 = rbf(acc[a][b][2]) + bf2f((u16)(rv[1] & 0xffff)), o3 = rbf(acc[a][b][3]) + bf2f((u16)(rv[1] >> 16));
          *(u32x2*)(outp + (size_t)m * ldc + n) = (u32x2){pack2(o0, o1), pack2(o2, o3)};
        }
      }
    }
  } else {
    constexpr int OC = EPI == E_GLU ? NWN * 32 : BN;
    constexpr int ES = EPI == E_SLAB ? 4 : 2;
    constexpr int RB = OC * ES, RS = RB + 16, CPR = RB / 16;
    lbar();
    if (comp) {
#pragma unroll
      for (int a = 0; a < 4; ++a) {
        char* rowp = smem + (wm * 64 + a * 16 + fr) * RS;
        if (EPI == E_GLU) {
#pragma unroll
          for (int b = 0; b < 2; ++b) {
            float o[4];
#pragma unroll
            for (int r = 0; r < 4; ++r) o[r] = rbf(acc[a][b][r]) * rbf(acc[a][b + 2][r]);
            *(u32x2*)(rowp + (wn * 32 + b * 16 + g * 4) * 2) = (u32x2){pack2(o[0], o[1]), pack2(o[2], o[3])};
          }
        } else {
#pragma unroll
          for (int b = 0; b < 4; ++b) {
            if (EPI == E_BF16) *(u32x2*)(rowp + (wn * 64 + b * 16 + g * 4) * 2) = (u32x2){pack2(acc[a][b][0], acc[a][b][1]), pack2(acc[a][b][2], acc[a][b][3])};
            else *(f32x4*)(rowp + (wn * 64 + b * 16 + g * 4) * 4) = acc[a][b];
          }
        }
      }
    }
    lbar();
#pragma unroll 2
    for (int c = tid; c < BM * CPR; c += NTH) {
      int row = c / CPR, ch = c % CPR;
      u32x4 v = *(const u32x4*)(smem + row * RS + ch * 16);
      st_sc1_16(rW, coff + (u32)((m0 + row) * ldc + nb) * ES + ch * 16, v);
    }
  }
}

__device__ __forceinline__ void attn_wave(const char* ws, rsrc_t rW, int s, int h, u16* vL) {
  int tz = threadIdx.x; asm volatile("" : "+v"(tz));
  const int lane = tz & 63, fr = lane & 15, g = lane >> 4, q4 = fr >> 2, p4 = fr & 3;
  const u16* qx = (const u16*)(ws + WS_QX); const u16* kx = (const u16*)(ws + WS_KX); const u16* vx = (const u16*)(ws + WS_VX);
  const u16* km = (const u16*)(ws + WS_KM); const u16* vm = (const u16*)(ws + WS_VM);
#pragma unroll
  for (int i = 0; i < 16; ++i) {
    int c = i * 64 + lane; int row = c / 25, ch = c % 25; if (row > 39) row = 39; if (ch > 23) ch = 23;
    const u16* sp = row < 8 ? vm + ((size_t)(s * M_ + row)) * HE_ : vx + ((size_t)(s * SEG_ + row - 8)) * HE_;
    __builtin_amdgcn_global_load_lds((const u32*)(sp + h * E_ + ch * 8), (LAS u32*)((char*)vL + i * 1024), 16, 0, 0);
  }
  f32x4 sc[3][2];
#pragma unroll
  for (int a = 0; a < 3; ++a) { sc[a][0] = (f32x4){0.f, 0.f, 0.f, 0.f}; sc[a][1] = (f32x4){0.f, 0.f, 0.f, 0.f}; }
  {
    const u16* kp[3];
#pragma unroll
    for (int a = 0; a < 3; ++a) {
      int key = a * 16 + fr; if (key > 39) key = 39;
      kp[a] = (key < 8 ? km + ((size_t)(s * M_ + key)) * HE_ : kx + ((size_t)(s * SEG_ + key - 8)) * HE_) + h * E_ + g * 8;
    }
    const u16* qp0 = qx + ((size_t)(s * SEG_ + fr)) * HE_ + h * E_ + g * 8;
    const u16* qp1 = qp0 + (size_t)16 * HE_;
#pragma unroll
    for (int hf = 0; hf < 2; ++hf) {
      bf16x8 qf[3][2], kf[3][3];
#pragma unroll
      for (int kk = 0; kk < 3; ++kk) {
        qf[kk][0] = *(const bf16x8*)(qp0 + (hf * 3 + kk) * 32); qf[kk][1] = *(const bf16x8*)(qp1 + (hf * 3 + kk) * 32);
#pragma unroll
        for (int a = 0; a < 3; ++a) kf[kk][a] = *(const bf16x8*)(kp[a] + (hf * 3 + kk) * 32);
      }
#pragma unroll
      for (int kk = 0; kk < 3; ++kk)
#pragma unroll
        for (int a = 0; a < 3; ++a) { sc[a][0] = mfma16(kf[kk][a], qf[kk][0], sc[a][0]); sc[a][1] = mfma16(kf[kk][a], qf[kk][1], sc[a][1]); }
    }
  }
  const float scale = 0.07216878364870322f;
  bf16x8 pb[2][2];
#pragma unroll
  for (int qt = 0; qt < 2; ++qt) {
    int qrow = qt * 16 + fr;
    float v[12]; float mx = -3.0e38f;
#pragma unroll
    for (int a = 0; a < 3; ++a)
#pragma unroll
      for (int r = 0; r < 4; ++r) {
        int key = a * 16 + g * 4 + r;
        float x = rbf(rbf(sc[a][qt][r]) * scale);
        bool vis = key < 8 || (key - 8) <= qrow;
        x = vis ? x : rbf(-1000000000.0f);
        v[a * 4 + r] = x;
        if (key < 40) mx = fmaxf(mx, x);
      }
    mx = red16_max(mx); mx = red32_max(mx);
    float sum = 0.f;
#pragma unroll
    for (int a = 0; a < 3; ++a)
#pragma unroll
      for (int r = 0; r < 4; ++r) {
        int key = a * 16 + g * 4 + r;
        float e = key < 40 ? __expf(v[a * 4 + r] - mx) : 0.f;
        v[a * 4 + r] = e; sum += e;
      }
    sum = red16_add(sum); sum = red32_add(sum);
    float inv = 1.0f / sum;
#pragma unroll
    for (int r = 0; r < 4; ++r) {
      pb[qt][0][r] = (short)f2bf(v[r] * inv); pb[qt][0][4 + r] = (short)f2bf(v[4 + r] * inv);
      pb[qt][1][r] = (short)f2bf(v[8 + r] * inv); pb[qt][1][4 + r] = 0;
    }
  }
  asm volatile("s_waitcnt vmcnt(0)" ::: "memory");
#pragma unroll
  for (int i = 0; i < 4; ++i) { int c = i * 64 + lane; if (c < 200) *(u32x4*)(vL + 40 * 200 + c * 8) = (u32x4){0u, 0u, 0u, 0u}; }
  asm volatile("s_waitcnt lgkmcnt(0)" ::: "memory");
  u32x2 ov[12][2];
#pragma unroll
  for (int et = 0; et < 12; ++et) {
    int r0 = g * 4 + q4;
    bf16x8 a0 = cat8(tr_read(vL + r0 * 200 + et * 16 + p4 * 4), tr_read(vL + (16 + r0) * 200 + et * 16 + p4 * 4));
    bf16x4 t2 = tr_read(vL + (32 + r0) * 200 + et * 16 + p4 * 4);
    bf16x8 a1 = cat8(t2, t2);
#pragma unroll
    for (int qt = 0; qt < 2; ++qt) {
      f32x4 acc = {0.f, 0.f, 0.f, 0.f};
      acc = mfma16(a0, pb[qt][0], acc);
      acc = mfma16(a1, pb[qt][1], acc);
      ov[et][qt] = (u32x2){pack2(acc[0], acc[1]), pack2(acc[2], acc[3])};
    }
  }
  asm volatile("s_waitcnt lgkmcnt(0)" ::: "memory");
#pragma unroll
  for (int et = 0; et < 12; ++et)
#pragma unroll
    for (int qt = 0; qt < 2; ++qt) *(u32x2*)(vL + (qt * 16 + fr) * 200 + et * 16 + g * 4) = ov[et][qt];
  asm volatile("s_waitcnt lgkmcnt(0)" ::: "memory");
#pragma unroll
  for (int i = 0; i < 12; ++i) {
    int c = i * 64 + lane; int row = c / 24, ch = c % 24;
    u32x4 v = *(const u32x4*)(vL + row * 200 + ch * 8);
    st_sc1_16(rW, WS_OX + (u32)((s * SEG_ + row) * HE_ + h * E_ + ch * 8) * 2, v);
  }
}

template <int NS>
__device__ __forceinline__ void redln_row(char* ws, rsrc_t rW, int row) {
  const FP p = getp();
  const int lane = threadIdx.x & 63;
  const bool two = lane < 16;
  float v[16];
#pragma unroll
  for (int hf = 0; hf < 2; ++hf) {
    int c0 = (hf == 0 ? lane : (two ? 64 + lane : lane)) * 8;
    const f32x4* sl = (const f32x4*)((const float*)(ws + WS_SLAB) + (size_t)row * D_ + c0);
    f32x4 lo[NS], hi[NS];
#pragma unroll
    for (int q = 0; q < NS; ++q) { lo[q] = sl[(size_t)q * (S_ * D_ / 4)]; hi[q] = sl[(size_t)q * (S_ * D_ / 4) + 1]; }
    f32x4 s0 = ((lo[0] + lo[1]) + (lo[2] + lo[3])) + ((lo[4] + lo[5]) + (lo[6] + lo[7]));
    f32x4 s1 = ((hi[0] + hi[1]) + (hi[2] + hi[3])) + ((hi[4] + hi[5]) + (hi[6] + hi[7]));
    if (NS == 12) { s0 += (lo[8 % NS] + lo[9 % NS]) + (lo[10 % NS] + lo[11 % NS]); s1 += (hi[8 % NS] + hi[9 % NS]) + (hi[10 % NS] + hi[11 % NS]); }
    u32x4 xw = *(const u32x4*)(p.x + (size_t)row * D_ + c0);
    u32x4 hw;
#pragma unroll
    for (int i = 0; i < 2; ++i) {
      hw[i] = pack2(rbf(s0[2 * i]) + bf2f((u16)(xw[i] & 0xffff)), rbf(s0[2 * i + 1]) + bf2f((u16)(xw[i] >> 16)));
      hw[2 + i] = pack2(rbf(s1[2 * i]) + bf2f((u16)(xw[2 + i] & 0xffff)), rbf(s1[2 * i + 1]) + bf2f((u16)(xw[2 + i] >> 16)));
    }
#pragma unroll
    for (int i = 0; i < 4; ++i) { v[hf * 8 + 2 * i] = bf2f((u16)(hw[i] & 0xffff)); v[hf * 8 + 2 * i + 1] = bf2f((u16)(hw[i] >> 16)); }
    if (hf == 0 || two) st_sc1_16(rW, WS_H + (u32)(row * D_ + c0) * 2, hw);
  }
  float sum = 0.f;
#pragma unroll
  for (int i = 0; i < 8; ++i) sum += v[i] + (two ? v[8 + i] : 0.f);
#pragma unroll
  for (int m = 32; m >= 1; m >>= 1) sum += __shfl_xor(sum, m);
  float mean = sum * (1.0f / D_), sq = 0.f;
#pragma unroll
  for (int i = 0; i < 8; ++i) { float d = v[i] - mean, e = v[8 + i] - mean; sq += d * d + (two ? e * e : 0.f); }
#pragma unroll
  for (int m = 32; m >= 1; m >>= 1) sq += __shfl_xor(sq, m);
  float mbf = rbf(mean), den = rbf(sqrtf(rbf(rbf(sq * (1.0f / D_)) + 1e-5f)));
#pragma unroll
  for (int hf = 0; hf < 2; ++hf) {
    u32x4 o;
#pragma unroll
    for (int i = 0; i < 4; ++i) o[i] = pack2(rbf(v[hf * 8 + 2 * i] - mbf) / den, rbf(v[hf * 8 + 2 * i + 1] - mbf) / den);
    if (hf == 0 || two) st_sc1_16(rW, WS_HN + (u32)(row * D_ + (hf == 0 ? lane : 64 + lane) * 8) * 2, o);
  }
}

template <bool FINE>
__device__ __forceinline__ void phase_b(int mb, int gb, char* ws, rsrc_t rW, char* smem) {
  const int wave = __builtin_amdgcn_readfirstlane(threadIdx.x >> 6);
  if (FINE || gb < 32) {
    const int hh = gb & 31;
    wait_flag(ws, WS_FQKV + (u32)(mb * 96 + 64 + hh) * 64, 1);
#pragma unroll 1
    for (int j = 0; j < 4; ++j) wait_flag(ws, WS_FCH + (u32)(hh * 4 + j) * 64, (u32)(mb + 1));
    acquire_bar();
    if (wave < (FINE ? 2 : 8)) attn_wave(ws, rW, mb * 8 + (FINE ? (gb >> 5) * 2 : 0) + wave, hh, (u16*)(smem + wave * 19200));
    signal_flag(ws, WS_FAT + (u32)mb * 64);
  }
  if (FINE) {
    if (gb < 120) {
      int mi = gb & 1, nt = (gb >> 1) % 5, ks = gb / 10;
      wait_flag(ws, WS_FAT + (u32)mb * 64, 128);
      acquire_bar();
      const FP p = getp();
      gtile<2, 2, 2, E_SLAB>((const u16*)(ws + WS_OX) + ks * 512, HE_, p.Wo + (size_t)ks * 512 * D_, nullptr, D_, 8, mb * 256 + mi * 128, nt * 128, rW,
                             WS_SLAB + (u32)ks * (S_ * D_ * 4), D_, nullptr, nullptr, smem);
      signal_flag(ws, WS_FOP + (u32)mb * 64);
    }
  } else if (gb >= 32 && gb < 72) {
    int t = gb - 32, nt = t % 5, ks = t / 5;
    wait_flag(ws, WS_FAT + (u32)mb * 64, 32);
    acquire_bar();
    const FP p = getp();
    gtile<4, 2, 1, E_SLAB>((const u16*)(ws + WS_OX) + ks * 768, HE_, p.Wo + (size_t)ks * 768 * D_, nullptr, D_, 12, mb * 256, nt * 128, rW,
                           WS_SLAB + (u32)ks * (S_ * D_ * 4), D_, nullptr, nullptr, smem);
    signal_flag(ws, WS_FOP + (u32)mb * 64);
  }
  if (FINE || (gb >= 32 && gb < 54)) {
    wait_flag(ws, WS_FOP + (u32)mb * 64, FINE ? 120 : 40);
    acquire_bar();
    const int rr = FINE ? gb * 2 + wave : (gb - 32) * 12 + wave;
    if (FINE ? wave < 2 : rr < 256) redln_row<FINE ? 12 : 8>(ws, rW, mb * 256 + rr);
    signal_flag(ws, WS_FLN + (u32)mb * 64);
  }
  if (FINE) {
    if (gb < 64) {
      wait_flag(ws, WS_FLN + (u32)mb * 64, 128);
      acquire_bar();
      const FP p = getp();
      gtile<1, 3, 2, E_GLU>((const u16*)(ws + WS_HN), D_, p.wg, p.wu, F_, 10, mb * 256 + (gb & 3) * 64, (gb >> 2) * 96, rW, WS_GU, F_, nullptr, nullptr, smem);
      signal_flag(ws, WS_FGL + (u32)mb * 64);
    } else if (gb < 104) {
      wait_flag(ws, WS_FGL + (u32)mb * 64, 64);
      acquire_bar();
      const FP p = getp();
      gtile<1, 1, 4, E_DOWN>((const u16*)(ws + WS_GU), F_, p.wd, nullptr, D_, 24, mb * 256 + ((gb - 64) & 3) * 64, ((gb - 64) >> 2) * 64, rW, 0, D_, (const u16*)(ws + WS_H), p.out, smem);
      __syncthreads();
    }
  } else {
    if (gb >= 72 && gb < 88) {
      wait_flag(ws, WS_FLN + (u32)mb * 64, 22);
      acquire_bar();
      const FP p = getp();
      gtile<4, 3, 1, E_GLU>((const u16*)(ws + WS_HN), D_, p.wg, p.wu, F_, 10, mb * 256, (gb - 72) * 96, rW, WS_GU, F_, nullptr, nullptr, smem);
      signal_flag(ws, WS_FGL + (u32)mb * 64);
    } else if (gb >= 88) {
      wait_flag(ws, WS_FGL + (u32)mb * 64, 16);
      acquire_bar();
      const FP p = getp();
      gtile<1, 1, 4, E_DOWN>((const u16*)(ws + WS_GU), F_, p.wd, nullptr, D_, 24, mb * 256 + ((gb - 88) & 3) * 64, ((gb - 88) >> 2) * 64, rW, 0, D_, (const u16*)(ws + WS_H), p.out, smem);
      __syncthreads();
    }
  }
}

__device__ __forceinline__ void gemm_role(int gb, char* smem) {
  char* ws = getp().ws;
  rsrc_t rW = __builtin_amdgcn_make_buffer_rsrc(ws, 0, WS_END, 0x00020000);
  {
    int n = gb >> 1, h = n & 31;
    const FP p = getp();
    gtile<2, 3, 2, E_BF16>((const u16*)(ws + WS_XN), D_, n < 32 ? p.Wk : p.Wv, nullptr, HE_, 10, (gb & 1) * 128, h * E_, rW, n < 32 ? WS_KX : WS_VX, HE_, nullptr, nullptr, smem);
    signal_flag(ws, WS_FQKV + (u32)n * 64);
  }
#pragma unroll 1
  for (int a = gb + 64; a < 768; a += 128) {
    int mb = a / 96, n = a % 96; int mat = n >> 5, h = n & 31;
    const FP p = getp();
    const u16* W = mat == 0 ? p.Wk : (mat == 1 ? p.Wv : p.Wq);
    u32 coff = mat == 0 ? WS_KX : (mat == 1 ? WS_VX : WS_QX);
    gtile<4, 3, 1, E_BF16>((const u16*)(ws + WS_XN), D_, W, W, HE_, 10, mb * 256, h * E_, rW, coff, HE_, nullptr, nullptr, smem);
    signal_flag(ws, WS_FQKV + (u32)(mb * 96 + n) * 64);
  }
#pragma unroll 1
  for (int mb = 0; mb < 7; ++mb) {
    phase_b<false>(mb, gb, ws, rW, smem);
  }
}

constexpr int MST = 656, QST = 80, VST = 208, PST = 72;
constexpr int CL_MEM = 0;
constexpr int CL_Q   = 8 * MST * 2;
constexpr int KBUF   = 48 * QST * 2;
constexpr int VBUF   = 40 * VST * 2;
constexpr int CL_K   = CL_Q + 8 * QST * 2;
constexpr int CL_V   = CL_K + 2 * KBUF;
constexpr int CL_P   = CL_V + 2 * VBUF;
constexpr int CL_F   = CL_P + 8 * PST * 2;
constexpr int CL_END = CL_F + 16;
constexpr int CL_STG = CL_Q;

constexpr int DLY_A2I = 0;
constexpr int DLY_S = 8;
constexpr int DLY_B = 4;
constexpr int DLY_V = 3;
constexpr int DLY_A1 = 0;
constexpr int DLY_A2 = 0;
constexpr int NPOLL = 11;

__device__ __forceinline__ void chain_role(int cu, char* smem) {
  const FP p = getp();
  const int h = cu >> 2, j = cu & 3;
  const int tid = threadIdx.x, lane = tid & 63;
  const int wave = __builtin_amdgcn_readfirstlane(tid >> 6);
  const int fr = lane & 15, g = lane >> 4, q4 = fr >> 2, p4 = fr & 3;
  char* ws = p.ws;
  u32* ctl = (u32*)(ws + WS_CTL);
  rsrc_t rW = __builtin_amdgcn_make_buffer_rsrc(ws, 0, WS_END, 0x00020000);
  const u32 a2base0 = WS_GA2 + (u32)(cu % A2_REP) * A2_STRIDE;

  bf16x8 Wf[20];
  bf16x8 Wof[6];
  {
#pragma unroll 1
    for (int rnd = 0; rnd < 2; ++rnd) {
      if (rnd) __syncthreads();
#pragma unroll 1
      for (int ii = wave; ii < 120; ii += 12) {
        int m2 = ii / 60, c = (ii % 60) * 64 + lane;
        const u16* sp;
        if (rnd == 1 && m2 == 1) { int e = c / 20, ch = c % 20; sp = p.Wo + (size_t)(h * E_ + e) * D_ + j * 160 + ch * 8; }
        else { const u16* W = rnd == 0 ? (m2 == 0 ? p.Wq : p.Wk) : p.Wv; int d = c / 6, ch = c % 6; sp = W + (size_t)d * HE_ + h * E_ + j * 48 + ch * 8; }
        __builtin_amdgcn_global_load_lds((const u32*)sp, (LAS u32*)(smem + ii * 1024), 16, 0, 0);
      }
      asm volatile("s_waitcnt vmcnt(0)" ::: "memory");
      __syncthreads();
      const int mat = wave / 3;
      if ((rnd == 0 && mat < 2) || (rnd == 1 && mat == 2)) {
        const u16* stg = (const u16*)(smem + (rnd == 0 ? mat : 0) * 61440);
        int e0 = (wave % 3) * 16;
#pragma unroll
        for (int ks = 0; ks < 20; ++ks) {
          int r0 = ks * 32 + g * 8 + q4;
          Wf[ks] = cat8(tr_read(stg + r0 * 48 + e0 + p4 * 4), tr_read(stg + (r0 + 4) * 48 + e0 + p4 * 4));
        }
      }
    }
    const u16* stg = (const u16*)(smem + 61440);
    const int wc = wave < 10 ? wave : 0;
#pragma unroll
    for (int t = 0; t < 6; ++t) {
      int r0 = t * 32 + g * 8 + q4;
      Wof[t] = cat8(tr_read(stg + r0 * 160 + wc * 16 + p4 * 4), tr_read(stg + (r0 + 4) * 160 + wc * 16 + p4 * 4));
    }
    __syncthreads();
  }
  for (int i = tid; i < (CL_END - CL_Q) / 4; i += NTH) ((u32*)(smem + CL_Q))[i] = 0u;
  __syncthreads();

  u16* memL = (u16*)(smem + CL_MEM);
  u16* qL = (u16*)(smem + CL_Q);
  u16* pL = (u16*)(smem + CL_P);
  volatile LAS u32* vflag = (volatile LAS u32*)(smem + CL_F);
  volatile LAS u32* pflag = vflag + 1;
  volatile LAS u32* rflag = vflag + 2;
  bool dead = false;

  auto xfetch = [&](int seg, int bf, int ii, int ln) {
    u32 off;
    if (ii < 5) { int c = ii * 64 + ln; int tok = c / 10, ch = c % 10;
      off = ch < 6 ? WS_KX + (u32)((seg * SEG_ + tok) * HE_ + h * E_ + j * 48 + ch * 8) * 2 : 2048u;
      __builtin_amdgcn_global_load_lds((const u32*)(ws + off), (LAS u32*)(smem + CL_K + bf * KBUF + 8 * QST * 2 + ii * 1024), 16, 0, 16);
    } else { int c = (ii - 5) * 64 + ln; int tok = c / 26, ch = c % 26; if (ch >= 24) ch = 0;
      off = WS_VX + (u32)((seg * SEG_ + tok) * HE_ + h * E_ + ch * 8) * 2;
      __builtin_amdgcn_global_load_lds((const u32*)(ws + off), (LAS u32*)(smem + CL_V + bf * VBUF + 8 * VST * 2 + (ii - 5) * 1024), 16, 0, 16);
    }
  };
  auto wait_kv = [&](int mb) {
    u32* fk = (u32*)(ws + WS_FQKV + (u32)(mb * 96 + h) * 64); u32* fv = (u32*)(ws + WS_FQKV + (u32)(mb * 96 + 32 + h) * 64);
    u32 spins = 0;
    for (;;) {
      u32 a = __hip_atomic_load(fk, RLX_AGENT), b = __hip_atomic_load(fv, RLX_AGENT);
      const u32 need = mb == 0 ? 2u : 1u;
      if (__builtin_amdgcn_readfirstlane(a) >= need && __builtin_amdgcn_readfirstlane(b) >= need) break;
      __builtin_amdgcn_s_sleep(4);
      if (dead || spin_fail(spins, ctl)) { dead = true; break; }
    }
    asm volatile("" ::: "memory");
  };
  wait_kv(0);
  for (int ii = wave; ii < 18; ii += 12) xfetch(0, 0, ii, lane);
  asm volatile("s_waitcnt vmcnt(0)" ::: "memory");
  __syncthreads();
  u32 vwx[4];
  auto calc_vwx = [&](int bf, int fr_, int g_) {
    const u16* vb = (const u16*)(smem + CL_V + bf * VBUF) + (8 + fr_) * VST + g_ * 8;
    f32x4 a0 = {0.f, 0.f, 0.f, 0.f}, a1 = {0.f, 0.f, 0.f, 0.f};
#pragma unroll
    for (int t = 0; t < 6; ++t) {
      a0 = mfma16(*(const bf16x8*)(vb + t * 32), Wof[t], a0);
      a1 = mfma16(*(const bf16x8*)(vb + 16 * VST + t * 32), Wof[t], a1);
    }
    vwx[0] = pack2(a0[0], a0[1]); vwx[1] = pack2(a0[2], a0[3]); vwx[2] = pack2(a1[0], a1[1]); vwx[3] = pack2(a1[2], a1[3]);
  };
  calc_vwx(0, fr, g);

#pragma unroll 1
  for (int s = 0; s < NSEG_; ++s) {
    int tz = threadIdx.x; asm volatile("" : "+v"(tz));
    const int tid = tz, lane = tz & 63, fr = lane & 15, g = lane >> 4;
    const u32 ep = (u32)s + 1u;
    const u32 par = ((u32)(s / RNG) + 1u) & 1u;
    const u32 WS_GB = WS_GB0 + (u32)(s % RNG) * GB_SZ, WS_GA1 = WS_GA10 + (u32)(s % RNG) * A1_SZ;
    const u32 a2w = (u32)(s % RNG) * A2_SZ, a2base = a2base0 + (u32)((s + RNG - 1) % RNG) * A2_SZ;
    const int buf = s & 1;
    u16* kL = (u16*)(smem + CL_K + buf * KBUF);
    u16* vL = (u16*)(smem + CL_V + buf * VBUF);
    if (s == 0) {
      for (int c = tid; c < 8 * 80; c += NTH) { int r = c / 80, ch = c % 80; *(u32x4*)(memL + r * MST + ch * 8) = *(const u32x4*)(getp().mem0 + r * D_ + ch * 8); }
    } else if (wave < 10) {
      const u32 epm = (u32)s;
      u32x4 a0, a1; u32 spins = 0;
      for (int i = 0; i < DLY_A2I; ++i) __builtin_amdgcn_s_sleep(4);
      for (;;) {
        asm volatile("" ::: "memory");
        a0 = ld_sc1_16(rW, a2base + tid * 16); a1 = ld_sc1_16(rW, a2base + (tid + 640) * 16);
        bool ok = a0[1] == epm && a0[3] == epm && a1[1] == epm && a1[3] == epm;
        if (__all(ok)) break;
        for (int i = 0; i < DLY_A2; ++i) __builtin_amdgcn_s_sleep(2);
        if (dead || spin_fail(spins, ctl)) { dead = true; break; }
      }
      { int u = tid; int row = ((u >> 4) & 1) * 4 + ((u >> 2) & 3), col = (u >> 5) * 16 + (u & 3) * 4; *(u32x2*)(memL + row * MST + col) = (u32x2){a0[0], a0[2]}; }
      { int u = tid + 640; int row = ((u >> 4) & 1) * 4 + ((u >> 2) & 3), col = (u >> 5) * 16 + (u & 3) * 4; *(u32x2*)(memL + row * MST + col) = (u32x2){a1[0], a1[2]}; }
    }
    lbar();
    if (wave < 9) {
      f32x4 acc = {0.f, 0.f, 0.f, 0.f};
      const u16* mb = memL + (fr & 7) * MST + g * 8;
      bf16x8 b0[4], b1[4];
#pragma unroll
      for (int i = 0; i < 4; ++i) b0[i] = *(const bf16x8*)(mb + i * 32);
#pragma unroll
      for (int gq = 0; gq < 5; ++gq) {
        if (gq & 1) {
          if (gq < 4) {
#pragma unroll
            for (int i = 0; i < 4; ++i) b0[i] = *(const bf16x8*)(mb + ((gq + 1) * 4 + i) * 32);
          }
#pragma unroll
          for (int i = 0; i < 4; ++i) acc = mfma16(Wf[gq * 4 + i], b1[i], acc);
        } else {
          if (gq < 4) {
#pragma unroll
            for (int i = 0; i < 4; ++i) b1[i] = *(const bf16x8*)(mb + ((gq + 1) * 4 + i) * 32);
          }
#pragma unroll
          for (int i = 0; i < 4; ++i) acc = mfma16(Wf[gq * 4 + i], b0[i], acc);
        }
        __builtin_amdgcn_sched_barrier(0);
      }
      u32x2 w = {pack2(acc[0], acc[1]), pack2(acc[2], acc[3])};
      int e = (wave % 3) * 16 + g * 4;
      if (fr < 8) {
        if (wave < 3) *(u32x2*)(qL + fr * QST + e) = w;
        else if (wave < 6) {
          *(u32x2*)(kL + fr * QST + e) = w;
          st_sc1_8(rW, WS_KM + (u32)((s * M_ + fr) * HE_ + h * E_ + j * 48 + e) * 2, w);
        } else {
          st_sc1_16(rW, WS_GB + (u32)(cu * 192 + 96 + (wave - 6) * 32 + fr * 4 + g) * 16, (u32x4){w[0], ep, w[1], ep});
          st_sc1_8(rW, WS_VM + (u32)((s * M_ + fr) * HE_ + h * E_ + j * 48 + e) * 2, w);
        }
      }
    }
    if (s == NSEG_ - 1) break;
    lbar();
    if (wave == 10) {
      if (((s + 1) & 7) == 0) wait_kv((s + 1) >> 3);
#pragma unroll 1
      for (int ii = 0; ii < 18; ++ii) xfetch(s + 1, buf ^ 1, ii, lane);
    }

    if (wave < 3) {
      f32x4 acc = {0.f, 0.f, 0.f, 0.f};
#pragma unroll
      for (int kk = 0; kk < 2; ++kk) {
        bf16x8 a = *(const bf16x8*)(kL + (wave * 16 + fr) * QST + kk * 32 + g * 8);
        bf16x8 b = *(const bf16x8*)(qL + (fr & 7) * QST + kk * 32 + g * 8);
        acc = mfma16(a, b, acc);
      }
      if (fr < 8)
        st_sc1_16(rW, WS_GB + (u32)(cu * 192 + wave * 32 + fr * 4 + g) * 16,
                  (u32x4){(__float_as_uint(acc[0]) & ~1u) | par, (__float_as_uint(acc[1]) & ~1u) | par,
                          (__float_as_uint(acc[2]) & ~1u) | par, (__float_as_uint(acc[3]) & ~1u) | par});
    }
    if (wave >= NPOLL) {
      const int r = lane & 7, q = lane >> 3;
      bool got = true;
      for (int i = 0; i < DLY_B; ++i) __builtin_amdgcn_s_sleep(4);
      const u32 o0 = WS_GB + (u32)(h * 4 * 192 + (q >> 2) * 32 + r * 4 + (q & 3)) * 16;
      const u32 o1 = WS_GB + (u32)(h * 4 * 192 + 64 + r * 4 + (q & 1)) * 16;
      u32x4 a[4], b[4]; u32 spins = 0;
      for (;;) {
        asm volatile("" ::: "memory");
        u32 aa = 0xffffffffu, oo = 0u;
#pragma unroll
        for (int i = 0; i < 4; ++i) {
          a[i] = __builtin_amdgcn_raw_buffer_load_b128(rW, o0, i * 3072, 16);
          b[i] = __builtin_amdgcn_raw_buffer_load_b128(rW, o1, i * 3072, 16);
        }
#pragma unroll
        for (int i = 0; i < 4; ++i) {
          aa &= a[i][0] & a[i][1] & a[i][2] & a[i][3] & b[i][0] & b[i][1] & b[i][2] & b[i][3];
          oo |= a[i][0] | a[i][1] | a[i][2] | a[i][3] | b[i][0] | b[i][1] | b[i][2] | b[i][3];
        }
        bool ok = (aa & 1u) == par && (oo & 1u) == par;
        if (__all(ok)) break;
        if (*pflag == ep) { got = false; break; }
        if (dead || spin_fail(spins, ctl)) { dead = true; break; }
      }
      if (got) {
      const float scale = 0.07216878364870322f;
      float sc[8];
#pragma unroll
      for (int k = 0; k < 4; ++k) {
        float x = (__uint_as_float(a[0][k]) + __uint_as_float(a[1][k])) + (__uint_as_float(a[2][k]) + __uint_as_float(a[3][k]));
        float y = (__uint_as_float(b[0][k]) + __uint_as_float(b[1][k])) + (__uint_as_float(b[2][k]) + __uint_as_float(b[3][k]));
        sc[k] = rbf(rbf(x) * scale); sc[4 + k] = rbf(rbf(y) * scale);
      }
      float mx = fmaxf(fmaxf(fmaxf(sc[0], sc[1]), fmaxf(sc[2], sc[3])), fmaxf(fmaxf(sc[4], sc[5]), fmaxf(sc[6], sc[7])));
      mx = fmaxf(mx, __uint_as_float(__builtin_amdgcn_update_dpp(0, __float_as_uint(mx), 0x128, 0xf, 0xf, false)));
      mx = red16_max(mx); mx = red32_max(mx);
      float sum = 0.f, sum2 = 0.f;
#pragma unroll
      for (int k = 0; k < 4; ++k) { sc[k] = __expf(sc[k] - mx); sum += sc[k]; sc[4 + k] = __expf(sc[4 + k] - mx); sum2 += sc[4 + k]; }
      if (q < 2) sum += sum2;
      sum += __uint_as_float(__builtin_amdgcn_update_dpp(0, __float_as_uint(sum), 0x128, 0xf, 0xf, false));
      sum = red16_add(sum); sum = red32_add(sum);
      float inv = __builtin_amdgcn_rcpf(sum);
      *(u32x2*)(pL + r * PST + q * 4) = (u32x2){pack2(sc[0] * inv, sc[1] * inv), pack2(sc[2] * inv, sc[3] * inv)};
      if (q < 2) *(u32x2*)(pL + r * PST + 32 + q * 4) = (u32x2){pack2(sc[4] * inv, sc[5] * inv), pack2(sc[6] * inv, sc[7] * inv)};
      asm volatile("s_waitcnt lgkmcnt(0)" ::: "memory");
      if (lane == 0) *pflag = ep;
      }
    } else if (wave == 9) {
      u32x4 v[6]; u32 spins = 0;
      for (int i = 0; i < DLY_V; ++i) __builtin_amdgcn_s_sleep(4);
      for (;;) {
        asm volatile("" ::: "memory");
        bool ok = true;
#pragma unroll
        for (int i = 0; i < 6; ++i) { int id = lane + 64 * i; int src = id / 96, u = id % 96; v[i] = ld_sc1_16(rW, WS_GB + (u32)((h * 4 + src) * 192 + 96 + u) * 16); }
#pragma unroll
        for (int i = 0; i < 6; ++i) ok = ok && v[i][1] == ep && v[i][3] == ep;
        if (__all(ok)) break;
        if (dead || spin_fail(spins, ctl)) { dead = true; break; }
      }
#pragma unroll
      for (int i = 0; i < 6; ++i) {
        int id = lane + 64 * i; int src = id / 96, u = id % 96; int vt = u >> 5, rr = (u >> 2) & 7, gg = u & 3;
        *(u32x2*)(vL + rr * VST + src * 48 + vt * 16 + gg * 4) = (u32x2){v[i][0], v[i][2]};
      }
      asm volatile("s_waitcnt lgkmcnt(0)" ::: "memory");
      if (lane == 0) *vflag = ep;
    }
    if (wave < 10) {
      while (*vflag != ep) { if (dead) break; }
      asm volatile("" ::: "memory");
      f32x4 am = {0.f, 0.f, 0.f, 0.f};
      const u16* vb = vL + (fr & 7) * VST + g * 8;
#pragma unroll
      for (int t = 0; t < 6; ++t) am = mfma16(*(const bf16x8*)(vb + t * 32), Wof[t], am);
      u32x4 a0u = {vwx[0], vwx[1], vwx[2], vwx[3]};
      u32x4 a1u = {g < 2 ? pack2(am[0], am[1]) : 0u, g < 2 ? pack2(am[2], am[3]) : 0u, 0u, 0u};
      while (*pflag != ep) { if (dead) break; }
      asm volatile("s_waitcnt vmcnt(0)" ::: "memory");
      const u16* pr = pL + (fr & 7) * PST;
      u32x2 x0 = *(const u32x2*)(pr + 8 + g * 4), x1 = *(const u32x2*)(pr + 24 + g * 4), x2 = *(const u32x2*)(pr + (g & 1) * 4);
      u32x4 b0u = {x0[0], x0[1], x1[0], x1[1]};
      u32x4 b1u = {g < 2 ? x2[0] : 0u, g < 2 ? x2[1] : 0u, 0u, 0u};
      f32x4 acc = {0.f, 0.f, 0.f, 0.f};
      acc = mfma16(__builtin_bit_cast(bf16x8, a0u), __builtin_bit_cast(bf16x8, b0u), acc);
      acc = mfma16(__builtin_bit_cast(bf16x8, a1u), __builtin_bit_cast(bf16x8, b1u), acc);
      if (fr < 8) {
        u32 o = WS_GA1 + (u32)(((j * 10 + wave) * 32 + h) * 32 + fr * 4 + g) * 16;
        st_sc1_16(rW, o, (u32x4){(__float_as_uint(acc[0]) & ~1u) | par, (__float_as_uint(acc[1]) & ~1u) | par,
                                 (__float_as_uint(acc[2]) & ~1u) | par, (__float_as_uint(acc[3]) & ~1u) | par});
      }
    }
    if (wave == 10) asm volatile("s_waitcnt vmcnt(0)" ::: "memory");
    lbar();
    if ((s & 7) == 7 && tid == 0) __hip_atomic_store((u32*)(ws + WS_FCH + (u32)cu * 64), (u32)(s >> 3) + 1u, RLX_AGENT);
    if (wave < 10) calc_vwx(buf ^ 1, fr, g);
    if (wave >= NPOLL) {
      if (cu < 80) {
        const int ch = lane & 15, hq = lane >> 4;
        bool got = true;
        for (int i = 0; i < DLY_A1; ++i) __builtin_amdgcn_s_sleep(4);
        const u32 rbase = WS_GA1 + (u32)(((cu >> 1) * 32 + hq * 8) * 32 + (cu & 1) * 16 + ch) * 16;
        u32x4 a[8]; u32 spins = 0;
        for (;;) {
          asm volatile("" ::: "memory");
          u32 aa = 0xffffffffu, oo = 0u;
#pragma unroll
          for (int i = 0; i < 8; ++i) a[i] = __builtin_amdgcn_raw_buffer_load_b128(rW, rbase, i * 512, 16);
#pragma unroll
          for (int i = 0; i < 8; ++i) { aa &= a[i][0] & a[i][1] & a[i][2] & a[i][3]; oo |= a[i][0] | a[i][1] | a[i][2] | a[i][3]; }
          bool ok = (aa & 1u) == par && (oo & 1u) == par;
          if (__all(ok)) break;
          if (*rflag == ep) { got = false; break; }
          if (dead || spin_fail(spins, ctl)) { dead = true; break; }
        }
        if (got) {
        float t[4];
#pragma unroll
        for (int k = 0; k < 4; ++k) {
          float x = ((__uint_as_float(a[0][k]) + __uint_as_float(a[1][k])) + (__uint_as_float(a[2][k]) + __uint_as_float(a[3][k])))
                  + ((__uint_as_float(a[4][k]) + __uint_as_float(a[5][k])) + (__uint_as_float(a[6][k]) + __uint_as_float(a[7][k])));
          x = red16_add(x); x = red32_add(x); t[k] = x;
        }
        u32 w0 = pack2(t[0], t[1]), w1 = pack2(t[2], t[3]);
#pragma unroll
        for (int it = 0; it < A2_REP / 4; ++it) st_sc1_16(rW, a2w + WS_GA2 + (u32)(cu * 16 + ch) * 16 + (u32)(hq + 4 * it) * A2_STRIDE, (u32x4){w0, ep, w1, ep});
        if (lane == 0) *rflag = ep;
        }
      }
      else if (wave == 11) {
        u32 spins = 0;
        for (int i = 0; i < DLY_S; ++i) __builtin_amdgcn_s_sleep(4);
        for (;;) {
          asm volatile("" ::: "memory");
          u32x4 a = ld_sc1_16(rW, a2base0 + a2w + (u32)(cu - 80) * 256);
          if (__all(a[1] == ep)) break;
          if (dead || spin_fail(spins, ctl)) { dead = true; break; }
        }
      }
    }
    lbar();
  }
  asm volatile("s_waitcnt vmcnt(0)" ::: "memory");
  __syncthreads();
  if (tid == 0) __hip_atomic_store((u32*)(ws + WS_FCH + (u32)cu * 64), 8u, RLX_AGENT);
  phase_b<true>(7, cu, ws, rW, smem);
}

__global__ __launch_bounds__(NTH, 3) void k_main(FP p) {
  extern __shared__ __attribute__((aligned(16))) char smem[];
  const int b = blockIdx.x;
  const int id = (b >> 4) * 8 + (b & 7);
  if ((b >> 3) & 1) gemm_role(id, smem);
  else chain_role(id, smem);
}

extern "C" void kernel_launch(void* const* d_in, const int* in_sizes, int n_in,
                              void* d_out, int out_size, void* d_ws, size_t ws_size,
                              hipStream_t stream) {
  char* ws = (char*)d_ws;
  (void)hipFuncSetAttribute((const void*)k_main, hipFuncAttributeMaxDynamicSharedMemorySize, LDS_BYTES);
  k_ln<<<S_ / 4, 256, 0, stream>>>((const u16*)d_in[0], (u16*)(ws + WS_XN), (u32x4*)ws);
  FP p{(const u16*)d_in[0], (const u16*)d_in[1], (const u16*)d_in[2], (const u16*)d_in[3], (const u16*)d_in[4], (const u16*)d_in[5],
       (const u16*)d_in[8], (const u16*)d_in[9], (const u16*)d_in[10], (u16*)d_out, ws};
  k_main<<<256, NTH, LDS_BYTES, stream>>>(p);
}
```

```cpp
#include <hip/hip_runtime.h>
#include <hip/hip_bf16.h>
#include <stdint.h>

typedef unsigned short u16;
typedef unsigned int u32;
typedef unsigned long long u64;
typedef short bf16x8 __attribute__((ext_vector_type(8)));
typedef short bf16x4 __attribute__((ext_vector_type(4)));
typedef float f32x4 __attribute__((ext_vector_type(4)));
typedef u32 u32x4 __attribute__((ext_vector_type(4)));
typedef u32 u32x2 __attribute__((ext_vector_type(2)));
#define LAS __attribute__((address_space(3)))
#define RLX_AGENT __ATOMIC_RELAXED, __HIP_MEMORY_SCOPE_AGENT

constexpr int S_ = 2048, D_ = 640, H_ = 32, E_ = 192, HE_ = H_ * E_;
constexpr int M_ = 8, SEG_ = 32, NSEG_ = 64, F_ = 1536;

constexpr u32 WS_CTL   = 0;
constexpr u32 WS_FQKV  = 4096;
constexpr u32 WS_FCH   = WS_FQKV + 8 * 96 * 64;
constexpr u32 WS_FAT   = WS_FCH + 128 * 64;
constexpr u32 WS_FOP   = WS_FAT + 64 * 64;
constexpr u32 WS_FLN   = WS_FOP + 32 * 64;
constexpr u32 WS_FGL   = WS_FLN + 32 * 64;
constexpr u32 WS_GA2   = (WS_FGL + 32 * 64 + 4095) / 4096 * 4096;
constexpr int A2R = 8;
constexpr int A2_REP = A2R, A2_STRIDE = 20736;
constexpr int RNG = 8;
constexpr u32 A2_SZ = A2_REP * A2_STRIDE, GB_SZ = 128 * 3072, A1_SZ = 40 * 32 * 512;
constexpr u32 WS_GB0   = WS_GA2 + RNG * A2_SZ;
constexpr u32 WS_GA10  = WS_GB0 + RNG * GB_SZ;
constexpr u32 WS_ZEND  = WS_GA10 + RNG * A1_SZ;
constexpr u32 WS_XN    = (WS_ZEND + 4095) / 4096 * 4096;
constexpr u32 WS_KX    = WS_XN + S_ * D_ * 2;
constexpr u32 WS_VX    = WS_KX + S_ * HE_ * 2;
constexpr u32 WS_QX    = WS_VX + S_ * HE_ * 2;
constexpr u32 WS_KM    = WS_QX + S_ * HE_ * 2;
constexpr u32 WS_VM    = WS_KM + NSEG_ * M_ * HE_ * 2;
constexpr u32 WS_OX    = WS_VM + NSEG_ * M_ * HE_ * 2;
constexpr u32 WS_H     = WS_OX + S_ * HE_ * 2;
constexpr u32 WS_HN    = WS_H + S_ * D_ * 2;
constexpr u32 WS_GU    = WS_HN + S_ * D_ * 2;
constexpr u32 WS_SLAB  = WS_GU + S_ * F_ * 2;
constexpr int NSLAB = 12;
constexpr u32 WS_END   = WS_SLAB + (u32)NSLAB * S_ * D_ * 4;

__device__ __forceinline__ float bf2f(u16 v) { return __uint_as_float(((u32)v) << 16); }
__device__ __forceinline__ u16 f2bf(float f) {
  __hip_bfloat16 b = __float2bfloat16(f);
  return *reinterpret_cast<u16*>(&b);
}
__device__ __forceinline__ float rbf(float f) { return bf2f(f2bf(f)); }
__device__ __forceinline__ u32 pack2(float a, float b) { return (u32)f2bf(a) | ((u32)f2bf(b) << 16); }
__device__ __forceinline__ f32x4 mfma16(bf16x8 a, bf16x8 b, f32x4 c) {
  return __builtin_amdgcn_mfma_f32_16x16x32_bf16(a, b, c, 0, 0, 0);
}
__device__ __forceinline__ bf16x4 tr_read(const void* p) {
  return __builtin_amdgcn_ds_read_tr16_b64_v4i16((LAS bf16x4*)p);
}
__device__ __forceinline__ bf16x8 cat8(bf16x4 a, bf16x4 b) {
  bf16x8 r; r[0]=a[0]; r[1]=a[1]; r[2]=a[2]; r[3]=a[3]; r[4]=b[0]; r[5]=b[1]; r[6]=b[2]; r[7]=b[3]; return r;
}
__device__ __forceinline__ void lbar() { asm volatile("s_waitcnt lgkmcnt(0)\n\ts_barrier" ::: "memory"); }
typedef __amdgpu_buffer_rsrc_t rsrc_t;
__device__ __forceinline__ u32x4 ld_sc1_16(rsrc_t r, u32 off) { return __builtin_amdgcn_raw_buffer_load_b128(r, off, 0, 16); }
__device__ __forceinline__ void st_sc1_16(rsrc_t r, u32 off, u32x4 v) { __builtin_amdgcn_raw_buffer_store_b128(v, r, off, 0, 16); }
__device__ __forceinline__ void st_sc1_8(rsrc_t r, u32 off, u32x2 v) { __builtin_amdgcn_raw_buffer_store_b64(v, r, off, 0, 16); }
__device__ __forceinline__ float red16_max(float x) {
  auto r = __builtin_amdgcn_permlane16_swap(__float_as_uint(x), __float_as_uint(x), false, false);
  return fmaxf(__uint_as_float(r[0]), __uint_as_float(r[1]));
}
__device__ __forceinline__ float red32_max(float x) {
  auto r = __builtin_amdgcn_permlane32_swap(__float_as_uint(x), __float_as_uint(x), false, false);
  return fmaxf(__uint_as_float(r[0]), __uint_as_float(r[1]));
}
__device__ __forceinline__ float red16_add(float x) {
  auto r = __builtin_amdgcn_permlane16_swap(__float_as_uint(x), __float_as_uint(x), false, false);
  return __uint_as_float(r[0]) + __uint_as_float(r[1]);
}
__device__ __forceinline__ float red32_add(float x) {
  auto r = __builtin_amdgcn_permlane32_swap(__float_as_uint(x), __float_as_uint(x), false, false);
  return __uint_as_float(r[0]) + __uint_as_float(r[1]);
}

constexpr u32 SPIN_CAP = 1u << 18;
__device__ __forceinline__ bool spin_fail(u32& spins, u32* ctl) {
  ++spins;
  if (spins > SPIN_CAP) { __hip_atomic_store(ctl, 1u, RLX_AGENT); return true; }
  return (spins & 255u) == 0u && __builtin_amdgcn_readfirstlane(__hip_atomic_load(ctl, RLX_AGENT)) != 0u;
}

struct FP {
  const u16* x; const u16* mem0; const u16* Wq; const u16* Wk; const u16* Wv; const u16* Wo;
  const u16* wg; const u16* wu; const u16* wd; u16* out; char* ws;
};
typedef const __attribute__((address_space(4))) FP* kargp_t;
__device__ __forceinline__ FP getp() {
#if defined(__HIP_DEVICE_COMPILE__)
  kargp_t kp = (kargp_t)__builtin_amdgcn_kernarg_segment_ptr(); asm volatile("" : "+s"(kp)); return *kp;
#else
  return FP{};
#endif
}
constexpr int NTH = 768;
constexpr int LDS_BYTES = 153600;

__device__ __forceinline__ void ln_row(const float (&v)[10], u32 (&o)[5]) {
  float sum = 0.f;
#pragma unroll
  for (int i = 0; i < 10; ++i) sum += v[i];
#pragma unroll
  for (int m = 32; m >= 1; m >>= 1) sum += __shfl_xor(sum, m);
  float mean = sum * (1.0f / D_);
  float sq = 0.f;
#pragma unroll
  for (int i = 0; i < 10; ++i) { float d = v[i] - mean; sq += d * d; }
#pragma unroll
  for (int m = 32; m >= 1; m >>= 1) sq += __shfl_xor(sq, m);
  float var = sq * (1.0f / D_);
  float mb = rbf(mean), vb = rbf(var);
  float den = rbf(sqrtf(rbf(vb + 1e-5f)));
#pragma unroll
  for (int i = 0; i < 5; ++i) o[i] = pack2(rbf(v[2 * i] - mb) / den, rbf(v[2 * i + 1] - mb) / den);
}
__global__ __launch_bounds__(256) void k_ln(const u16* __restrict__ x, u16* __restrict__ y, u32x4* __restrict__ zero,
                                            const u32x4* __restrict__ w0, const u32x4* __restrict__ w1, const u32x4* __restrict__ w2, const u32x4* __restrict__ w3) {
  {
    u32 acc = 0;
    for (u32 i = blockIdx.x * 256 + threadIdx.x; i < (u32)(D_ * HE_ / 8); i += (S_ / 4) * 256) {
      u32x4 a = w0[i], b = w1[i], c = w2[i], d = w3[i];
      acc ^= a[0] ^ b[1] ^ c[2] ^ d[3];
    }
    asm volatile("" :: "v"(acc));
  }
  for (u32 i = blockIdx.x * 256 + threadIdx.x; i < WS_ZEND / 16; i += (S_ / 4) * 256) zero[i] = (u32x4){0u, 0u, 0u, 0u};
  int row = blockIdx.x * 4 + (threadIdx.x >> 6);
  int lane = threadIdx.x & 63;
  const u32* xr = (const u32*)(x + (size_t)row * D_);
  float v[10];
#pragma unroll
  for (int i = 0; i < 5; ++i) { u32 w = xr[i * 64 + lane]; v[2 * i] = bf2f((u16)(w & 0xffff)); v[2 * i + 1] = bf2f((u16)(w >> 16)); }
  u32 o[5]; ln_row(v, o);
  u32* yr = (u32*)(y + (size_t)row * D_);
#pragma unroll
  for (int i = 0; i < 5; ++i) yr[i * 64 + lane] = o[i];
}

__device__ __forceinline__ void wait_flag(char* ws, u32 foff, u32 target) {
  if (threadIdx.x == 0) {
    u32* f = (u32*)(ws + foff); u32* ctl = (u32*)(ws + WS_CTL); u32 spins = 0;
    while (__hip_atomic_load(f, RLX_AGENT) < target) {
      __builtin_amdgcn_s_sleep(8);
      if (++spins > (1u << 20)) { __hip_atomic_store(ctl, 1u, RLX_AGENT); break; }
      if ((spins & 63u) == 0u && __hip_atomic_load(ctl, RLX_AGENT) != 0u) break;
    }
  }
}
__device__ __forceinline__ void acquire_bar() {
  if (threadIdx.x == 0) { __builtin_amdgcn_fence(__ATOMIC_ACQUIRE, "agent"); asm volatile("s_waitcnt vmcnt(0)" ::: "memory"); }
  __syncthreads();
}
__device__ __forceinline__ void signal_flag(char* ws, u32 foff) {
  asm volatile("s_waitcnt vmcnt(0)" ::: "memory");
  __syncthreads();
  if (threadIdx.x == 0) __hip_atomic_fetch_add((u32*)(ws + foff), 1u, RLX_AGENT);
}

template <int NWN> __device__ __forceinline__ int bswz(int row) {
  if (NWN == 3) return (((row >> 1) & 1) | (((row >> 3) & 1) << 1)) << 1;
  else if (NWN == 2) return ((row & 3) << 2) | ((row >> 2) & 3);
  else return (((row >> 1) & 1) | (((row >> 3) & 1) << 1)) << 1;
}
enum { E_BF16 = 0, E_SLAB = 1, E_GLU = 2, E_DOWN = 3 };
template <int NWM, int NWN, int KG, int EPI>
__device__ __forceinline__ void gtile(const u16* __restrict__ A, int lda, const u16* __restrict__ B0, const u16* __restrict__ B1, int ldb,
                                      int nk, int m0, int nb, rsrc_t rW, u32 coff, int ldc, const u16* resid, u16* outp, char* smem) {
  constexpr int BM = NWM * 64, BN = NWN * 64, BROW = BN * 2, NA = BM / 16, NI = NA + BN / 16, NT = NI * KG, ST_BYTES = NT * 1024;
  constexpr int NST = (143360 / ST_BYTES) < 16 ? (143360 / ST_BYTES) : 16, PD = NST - 1;
  constexpr int NPW = (NT + 11) / 12;
  constexpr int NW = NWM * NWN;
  int tz = threadIdx.x; asm volatile("" : "+v"(tz));
  const int tid = tz, lane = tid & 63, wave = __builtin_amdgcn_readfirstlane(tid >> 6);
  const int kg = wave / NW, wv = wave % NW, wm = wv % NWM, wn = wv / NWM;
  const int fr = lane & 15, g = lane >> 4;
  const u16* src[NPW]; u32 sstep[NPW];
#pragma unroll
  for (int i = 0; i < NPW; ++i) {
    int it = wave + 12 * i; if (it >= NT) it = 0;
    int sub = it / NI, ii = it % NI;
    if (ii < NA) { int b = ii * 1024 + lane * 16; int row = b >> 6, pos = (b >> 4) & 3, ch = pos ^ ((4 - (row >> 2)) & 3);
      src[i] = A + (size_t)(m0 + row) * lda + ch * 8 + sub * 32; sstep[i] = 32 * KG; }
    else { int b = (ii - NA) * 1024 + lane * 16; int row = b / BROW, pos = (b % BROW) >> 4;
      int ch = pos ^ bswz<NWN>(row);
      if (EPI == E_GLU) { int w2 = ch >> 3, within = ch & 7; const u16* base = (within >> 2) ? B1 : B0;
        src[i] = base + (size_t)(row + sub * 32) * ldb + nb + w2 * 32 + (within & 3) * 8; }
      else src[i] = B0 + (size_t)(row + sub * 32) * ldb + nb + ch * 8;
      sstep[i] = 32u * KG * (u32)ldb; }
  }
  const int npw = (wave + 12 * (NPW - 1)) < NT ? NPW : NPW - 1;
  auto stage = [&](int kt, int buf) {
    char* sb = smem + buf * ST_BYTES;
#pragma unroll
    for (int i = 0; i < NPW; ++i)
      if (i < npw) __builtin_amdgcn_global_load_lds((const u32*)(src[i] + (size_t)kt * sstep[i]), (LAS u32*)(sb + (wave + 12 * i) * 1024), 16, 0, 0);
  };
  f32x4 acc[4][4];
#pragma unroll
  for (int a = 0; a < 4; ++a)
#pragma unroll
    for (int b = 0; b < 4; ++b) acc[a][b] = (f32x4){0.f, 0.f, 0.f, 0.f};
  const bool compk = wave < NW * KG;
  const bool comp = wave < NW;
  const int nst = nk * 2 / KG;
#pragma unroll 1
  for (int i = 0; i < PD; ++i) if (i < nst) stage(i, i);
  int cb = 0, pb = PD;
  const int q = fr >> 2, pp = fr & 3;
  const int r0 = g * 8 + q, r1 = r0 + 4;
  const int x0 = bswz<NWN>(r0), x1 = bswz<NWN>(r1);
  const int aoff = kg * NI * 1024 + (wm * 64 + fr) * 64 + ((g ^ ((4 - (fr >> 2)) & 3)) << 4);
#pragma unroll 1
  for (int kt = 0; kt < nst; ++kt) {
    if (kt + PD <= nst) {
      if (npw == NPW) asm volatile("s_waitcnt vmcnt(%0)" :: "i"(NPW * (PD - 1)) : "memory");
      else asm volatile("s_waitcnt vmcnt(%0)" :: "i"((NPW - 1) * (PD - 1)) : "memory");
    }
    else asm volatile("s_waitcnt vmcnt(0)" ::: "memory");
    lbar();
    if (kt + PD < nst) stage(kt + PD, pb);
    if (compk) {
      const char* sa = smem + cb * ST_BYTES; const char* sb = sa + (kg * NI + NA) * 1024;
      bf16x8 af[4], bfr[4];
#pragma unroll
      for (int a = 0; a < 4; ++a) af[a] = *(const bf16x8*)(sa + aoff + a * 1024);
#pragma unroll
      for (int b = 0; b < 4; ++b) {
        int ch = wn * 8 + b * 2 + (pp >> 1);
        bfr[b] = cat8(tr_read(sb + r0 * BROW + ((ch ^ x0) << 4) + 8 * (pp & 1)), tr_read(sb + r1 * BROW + ((ch ^ x1) << 4) + 8 * (pp & 1)));
      }
#pragma unroll
      for (int a = 0; a < 4; ++a)
#pragma unroll
        for (int b = 0; b < 4; ++b) acc[a][b] = mfma16(bfr[b], af[a], acc[a][b]);
    }
    cb = cb == NST - 1 ? 0 : cb + 1; pb = pb == NST - 1 ? 0 : pb + 1;
  }
  if (KG > 1) {
    lbar();
    if (compk && kg > 0) {
      f32x4* d = (f32x4*)(smem + (wave - NW) * 16384) + lane;
#pragma unroll
      for (int a = 0; a < 4; ++a)
#pragma unroll
        for (int b = 0; b < 4; ++b) d[(a * 4 + b) * 64] = acc[a][b];
    }
    lbar();
    if (comp) {
#pragma unroll
      for (int k2 = 1; k2 < KG; ++k2) {
        const f32x4* d = (const f32x4*)(smem + ((k2 - 1) * NW + wv) * 16384) + lane;
#pragma unroll
        for (int a = 0; a < 4; ++a)
#pragma unroll
          for (int b = 0; b < 4; ++b) acc[a][b] += d[(a * 4 + b) * 64];
      }
    }
  }
  if (EPI == E_DOWN) {
    if (comp) {
#pragma unroll
      for (int a = 0; a < 4; ++a) {
        const int m = m0 + wm * 64 + a * 16 + fr;
#pragma unroll
        for (int b = 0; b < 4; ++b) {
          int n = nb + wn * 64 + b * 16 + g * 4;
          u32x2 rv = *(const u32x2*)(resid + (size_t)m * ldc + n);
          float o0 = rbf(acc[a][b][0]) + bf2f((u16)(rv[0] & 0xffff)), o1 = rbf(acc[a][b][1]) + bf2f((u16)(rv[0] >> 16));
          float o2 = rbf(acc[a][b][2]) + bf2f((u16)(rv[1] & 0xffff)), o3 = rbf(acc[a][b][3]) + bf2f((u16)(rv[1] >> 16));
          *(u32x2*)(outp + (size_t)m * ldc + n) = (u32x2){pack2(o0, o1), pack2(o2, o3)};
        }
      }
    }
  } else {
    constexpr int OC = EPI == E_GLU ? NWN * 32 : BN;
    constexpr int ES = EPI == E_SLAB ? 4 : 2;
    constexpr int RB = OC * ES, RS = RB + 16, CPR = RB / 16;
    lbar();
    if (comp) {
#pragma unroll
      for (int a = 0; a < 4; ++a) {
        char* rowp = smem + (wm * 64 + a * 16 + fr) * RS;
        if (EPI == E_GLU) {
#pragma unroll
          for (int b = 0; b < 2; ++b) {
            float o[4];
#pragma unroll
            for (int r = 0; r < 4; ++r) o[r] = rbf(acc[a][b][r]) * rbf(acc[a][b + 2][r]);
            *(u32x2*)(rowp + (wn * 32 + b * 16 + g * 4) * 2) = (u32x2){pack2(o[0], o[1]), pack2(o[2], o[3])};
          }
        } else {
#pragma unroll
          for (int b = 0; b < 4; ++b) {
            if (EPI == E_BF16) *(u32x2*)(rowp + (wn * 64 + b * 16 + g * 4) * 2) = (u32x2){pack2(acc[a][b][0], acc[a][b][1]), pack2(acc[a][b][2], acc[a][b][3])};
            else *(f32x4*)(rowp + (wn * 64 + b * 16 + g * 4) * 4) = acc[a][b];
          }
        }
      }
    }
    lbar();
#pragma unroll 2
    for (int c = tid; c < BM * CPR; c += NTH) {
      int row = c / CPR, ch = c % CPR;
      u32x4 v = *(const u32x4*)(smem + row * RS + ch * 16);
      st_sc1_16(rW, coff + (u32)((m0 + row) * ldc + nb) * ES + ch * 16, v);
    }
  }
}

__device__ __forceinline__ void attn_wave(const char* ws, rsrc_t rW, int s, int h, u16* vL) {
  int tz = threadIdx.x; asm volatile("" : "+v"(tz));
  const int lane = tz & 63, fr = lane & 15, g = lane >> 4, q4 = fr >> 2, p4 = fr & 3;
  const u16* qx = (const u16*)(ws + WS_QX); const u16* kx = (const u16*)(ws + WS_KX); const u16* vx = (const u16*)(ws + WS_VX);
  const u16* km = (const u16*)(ws + WS_KM); const u16* vm = (const u16*)(ws + WS_VM);
#pragma unroll
  for (int i = 0; i < 16; ++i) {
    int c = i * 64 + lane; int row = c / 25, ch = c % 25; if (row > 39) row = 39; if (ch > 23) ch = 23;
    const u16* sp = row < 8 ? vm + ((size_t)(s * M_ + row)) * HE_ : vx + ((size_t)(s * SEG_ + row - 8)) * HE_;
    __builtin_amdgcn_global_load_lds((const u32*)(sp + h * E_ + ch * 8), (LAS u32*)((char*)vL + i * 1024), 16, 0, 0);
  }
  f32x4 sc[3][2];
#pragma unroll
  for (int a = 0; a < 3; ++a) { sc[a][0] = (f32x4){0.f, 0.f, 0.f, 0.f}; sc[a][1] = (f32x4){0.f, 0.f, 0.f, 0.f}; }
  {
    const u16* kp[3];
#pragma unroll
    for (int a = 0; a < 3; ++a) {
      int key = a * 16 + fr; if (key > 39) key = 39;
      kp[a] = (key < 8 ? km + ((size_t)(s * M_ + key)) * HE_ : kx + ((size_t)(s * SEG_ + key - 8)) * HE_) + h * E_ + g * 8;
    }
    const u16* qp0 = qx + ((size_t)(s * SEG_ + fr)) * HE_ + h * E_ + g * 8;
    const u16* qp1 = qp0 + (size_t)16 * HE_;
#pragma unroll
    for (int hf = 0; hf < 2; ++hf) {
      bf16x8 qf[3][2], kf[3][3];
#pragma unroll
      for (int kk = 0; kk < 3; ++kk) {
        qf[kk][0] = *(const bf16x8*)(qp0 + (hf * 3 + kk) * 32); qf[kk][1] = *(const bf16x8*)(qp1 + (hf * 3 + kk) * 32);
#pragma unroll
        for (int a = 0; a < 3; ++a) kf[kk][a] = *(const bf16x8*)(kp[a] + (hf * 3 + kk) * 32);
      }
#pragma unroll
      for (int kk = 0; kk < 3; ++kk)
#pragma unroll
        for (int a = 0; a < 3; ++a) { sc[a][0] = mfma16(kf[kk][a], qf[kk][0], sc[a][0]); sc[a][1] = mfma16(kf[kk][a], qf[kk][1], sc[a][1]); }
    }
  }
  const float scale = 0.07216878364870322f;
  bf16x8 pb[2][2];
#pragma unroll
  for (int qt = 0; qt < 2; ++qt) {
    int qrow = qt * 16 + fr;
    float v[12]; float mx = -3.0e38f;
#pragma unroll
    for (int a = 0; a < 3; ++a)
#pragma unroll
      for (int r = 0; r < 4; ++r) {
        int key = a * 16 + g * 4 + r;
        float x = rbf(rbf(sc[a][qt][r]) * scale);
        bool vis = key < 8 || (key - 8) <= qrow;
        x = vis ? x : rbf(-1000000000.0f);
        v[a * 4 + r] = x;
        if (key < 40) mx = fmaxf(mx, x);
      }
    mx = red16_max(mx); mx = red32_max(mx);
    float sum = 0.f;
#pragma unroll
    for (int a = 0; a < 3; ++a)
#pragma unroll
      for (int r = 0; r < 4; ++r) {
        int key = a * 16 + g * 4 + r;
        float e = key < 40 ? __expf(v[a * 4 + r] - mx) : 0.f;
        v[a * 4 + r] = e; sum += e;
      }
    sum = red16_add(sum); sum = red32_add(sum);
    float inv = 1.0f / sum;
#pragma unroll
    for (int r = 0; r < 4; ++r) {
      pb[qt][0][r] = (short)f2bf(v[r] * inv); pb[qt][0][4 + r] = (short)f2bf(v[4 + r] * inv);
      pb[qt][1][r] = (short)f2bf(v[8 + r] * inv); pb[qt][1][4 + r] = 0;
    }
  }
  asm volatile("s_waitcnt vmcnt(0)" ::: "memory");
#pragma unroll
  for (int i = 0; i < 4; ++i) { int c = i * 64 + lane; if (c < 200) *(u32x4*)(vL + 40 * 200 + c * 8) = (u32x4){0u, 0u, 0u, 0u}; }
  asm volatile("s_waitcnt lgkmcnt(0)" ::: "memory");
  u32x2 ov[12][2];
#pragma unroll
  for (int et = 0; et < 12; ++et) {
    int r0 = g * 4 + q4;
    bf16x8 a0 = cat8(tr_read(vL + r0 * 200 + et * 16 + p4 * 4), tr_read(vL + (16 + r0) * 200 + et * 16 + p4 * 4));
    bf16x4 t2 = tr_read(vL + (32 + r0) * 200 + et * 16 + p4 * 4);
    bf16x8 a1 = cat8(t2, t2);
#pragma unroll
    for (int qt = 0; qt < 2; ++qt) {
      f32x4 acc = {0.f, 0.f, 0.f, 0.f};
      acc = mfma16(a0, pb[qt][0], acc);
      acc = mfma16(a1, pb[qt][1], acc);
      ov[et][qt] = (u32x2){pack2(acc[0], acc[1]), pack2(acc[2], acc[3])};
    }
  }
  asm volatile("s_waitcnt lgkmcnt(0)" ::: "memory");
#pragma unroll
  for (int et = 0; et < 12; ++et)
#pragma unroll
    for (int qt = 0; qt < 2; ++qt) *(u32x2*)(vL + (qt * 16 + fr) * 200 + et * 16 + g * 4) = ov[et][qt];
  asm volatile("s_waitcnt lgkmcnt(0)" ::: "memory");
#pragma unroll
  for (int i = 0; i < 12; ++i) {
    int c = i * 64 + lane; int row = c / 24, ch = c % 24;
    u32x4 v = *(const u32x4*)(vL + row * 200 + ch * 8);
    st_sc1_16(rW, WS_OX + (u32)((s * SEG_ + row) * HE_ + h * E_ + ch * 8) * 2, v);
  }
}

template <int NS>
__device__ __forceinline__ void redln_row(char* ws, rsrc_t rW, int row) {
  const FP p = getp();
  const int lane = threadIdx.x & 63;
  const bool two = lane < 16;
  float v[16];
#pragma unroll
  for (int hf = 0; hf < 2; ++hf) {
    int c0 = (hf == 0 ? lane : (two ? 64 + lane : lane)) * 8;
    const f32x4* sl = (const f32x4*)((const float*)(ws + WS_SLAB) + (size_t)row * D_ + c0);
    f32x4 lo[NS], hi[NS];
#pragma unroll
    for (int q = 0; q < NS; ++q) { lo[q] = sl[(size_t)q * (S_ * D_ / 4)]; hi[q] = sl[(size_t)q * (S_ * D_ / 4) + 1]; }
    f32x4 s0 = ((lo[0] + lo[1]) + (lo[2] + lo[3])) + ((lo[4] + lo[5]) + (lo[6] + lo[7]));
    f32x4 s1 = ((hi[0] + hi[1]) + (hi[2] + hi[3])) + ((hi[4] + hi[5]) + (hi[6] + hi[7]));
    if (NS == 12) { s0 += (lo[8 % NS] + lo[9 % NS]) + (lo[10 % NS] + lo[11 % NS]); s1 += (hi[8 % NS] + hi[9 % NS]) + (hi[10 % NS] + hi[11 % NS]); }
    u32x4 xw = *(const u32x4*)(p.x + (size_t)row * D_ + c0);
    u32x4 hw;
#pragma unroll
    for (int i = 0; i < 2; ++i) {
      hw[i] = pack2(rbf(s0[2 * i]) + bf2f((u16)(xw[i] & 0xffff)), rbf(s0[2 * i + 1]) + bf2f((u16)(xw[i] >> 16)));
      hw[2 + i] = pack2(rbf(s1[2 * i]) + bf2f((u16)(xw[2 + i] & 0xffff)), rbf(s1[2 * i + 1]) + bf2f((u16)(xw[2 + i] >> 16)));
    }
#pragma unroll
    for (int i = 0; i < 4; ++i) { v[hf * 8 + 2 * i] = bf2f((u16)(hw[i] & 0xffff)); v[hf * 8 + 2 * i + 1] = bf2f((u16)(hw[i] >> 16)); }
    if (hf == 0 || two) st_sc1_16(rW, WS_H + (u32)(row * D_ + c0) * 2, hw);
  }
  float sum = 0.f;
#pragma unroll
  for (int i = 0; i < 8; ++i) sum += v[i] + (two ? v[8 + i] : 0.f);
#pragma unroll
  for (int m = 32; m >= 1; m >>= 1) sum += __shfl_xor(sum, m);
  float mean = sum * (1.0f / D_), sq = 0.f;
#pragma unroll
  for (int i = 0; i < 8; ++i) { float d = v[i] - mean, e = v[8 + i] - mean; sq += d * d + (two ? e * e : 0.f); }
#pragma unroll
  for (int m = 32; m >= 1; m >>= 1) sq += __shfl_xor(sq, m);
  float mbf = rbf(mean), den = rbf(sqrtf(rbf(rbf(sq * (1.0f / D_)) + 1e-5f)));
#pragma unroll
  for (int hf = 0; hf < 2; ++hf) {
    u32x4 o;
#pragma unroll
    for (int i = 0; i < 4; ++i) o[i] = pack2(rbf(v[hf * 8 + 2 * i] - mbf) / den, rbf(v[hf * 8 + 2 * i + 1] - mbf) / den);
    if (hf == 0 || two) st_sc1_16(rW, WS_HN + (u32)(row * D_ + (hf == 0 ? lane : 64 + lane) * 8) * 2, o);
  }
}

template <bool FINE>
__device__ __forceinline__ void phase_b(int mb, int gb, char* ws, rsrc_t rW, char* smem) {
  const int wave = __builtin_amdgcn_readfirstlane(threadIdx.x >> 6);
  if (FINE || gb < 32) {
    const int hh = gb & 31;
    wait_flag(ws, WS_FQKV + (u32)(mb * 96 + 64 + hh) * 64, 1);
#pragma unroll 1
    for (int j = 0; j < 4; ++j) wait_flag(ws, WS_FCH + (u32)(hh * 4 + j) * 64, (u32)(mb + 1));
    acquire_bar();
    if (wave < (FINE ? 2 : 8)) attn_wave(ws, rW, mb * 8 + (FINE ? (gb >> 5) * 2 : 0) + wave, hh, (u16*)(smem + wave * 19200));
    signal_flag(ws, WS_FAT + (u32)mb * 64);
  }
  if (FINE) {
    if (gb < 120) {
      int mi = gb & 1, nt = (gb >> 1) % 5, ks = gb / 10;
      wait_flag(ws, WS_FAT + (u32)mb * 64, 128);
      acquire_bar();
      const FP p = getp();
      gtile<2, 2, 2, E_SLAB>((const u16*)(ws + WS_OX) + ks * 512, HE_, p.Wo + (size_t)ks * 512 * D_, nullptr, D_, 8, mb * 256 + mi * 128, nt * 128, rW,
                             WS_SLAB + (u32)ks * (S_ * D_ * 4), D_, nullptr, nullptr, smem);
      signal_flag(ws, WS_FOP + (u32)(mb * 4 + mi) * 64);
    }
  } else if (gb >= 32 && gb < 72) {
    int t = gb - 32, nt = t % 5, ks = t / 5;
    wait_flag(ws, WS_FAT + (u32)mb * 64, 32);
    acquire_bar();
    const FP p = getp();
    gtile<4, 2, 1, E_SLAB>((const u16*)(ws + WS_OX) + ks * 768, HE_, p.Wo + (size_t)ks * 768 * D_, nullptr, D_, 12, mb * 256, nt * 128, rW,
                           WS_SLAB + (u32)ks * (S_ * D_ * 4), D_, nullptr, nullptr, smem);
    signal_flag(ws, WS_FOP + (u32)(mb * 4) * 64);
  }
  if (FINE || (gb >= 32 && gb < 54)) {
    wait_flag(ws, WS_FOP + (u32)(mb * 4 + (FINE ? gb >> 6 : 0)) * 64, FINE ? 60 : 40);
    acquire_bar();
    const int rr = FINE ? gb * 2 + wave : (gb - 32) * 12 + wave;
    if (FINE ? wave < 2 : rr < 256) redln_row<FINE ? 12 : 8>(ws, rW, mb * 256 + rr);
    signal_flag(ws, WS_FLN + (u32)(mb * 4 + (FINE ? gb >> 5 : 0)) * 64);
  }
  if (FINE) {
    if (gb < 64) {
      wait_flag(ws, WS_FLN + (u32)(mb * 4 + (gb & 3)) * 64, 32);
      acquire_bar();
      const FP p = getp();
      gtile<1, 3, 2, E_GLU>((const u16*)(ws + WS_HN), D_, p.wg, p.wu, F_, 10, mb * 256 + (gb & 3) * 64, (gb >> 2) * 96, rW, WS_GU, F_, nullptr, nullptr, smem);
      signal_flag(ws, WS_FGL + (u32)(mb * 4 + (gb & 3)) * 64);
    } else if (gb < 104) {
      wait_flag(ws, WS_FGL + (u32)(mb * 4 + ((gb - 64) & 3)) * 64, 16);
      acquire_bar();
      const FP p = getp();
      gtile<1, 1, 4, E_DOWN>((const u16*)(ws + WS_GU), F_, p.wd, nullptr, D_, 24, mb * 256 + ((gb - 64) & 3) * 64, ((gb - 64) >> 2) * 64, rW, 0, D_, (const u16*)(ws + WS_H), p.out, smem);
      __syncthreads();
    }
  } else {
    if (gb >= 72 && gb < 88) {
      wait_flag(ws, WS_FLN + (u32)(mb * 4) * 64, 22);
      acquire_bar();
      const FP p = getp();
      gtile<4, 3, 1, E_GLU>((const u16*)(ws + WS_HN), D_, p.wg, p.wu, F_, 10, mb * 256, (gb - 72) * 96, rW, WS_GU, F_, nullptr, nullptr, smem);
      signal_flag(ws, WS_FGL + (u32)(mb * 4) * 64);
    } else if (gb >= 88) {
      wait_flag(ws, WS_FGL + (u32)(mb * 4) * 64, 16);
      acquire_bar();
      const FP p = getp();
      gtile<1, 1, 4, E_DOWN>((const u16*)(ws + WS_GU), F_, p.wd, nullptr, D_, 24, mb * 256 + ((gb - 88) & 3) * 64, ((gb - 88) >> 2) * 64, rW, 0, D_, (const u16*)(ws + WS_H), p.out, smem);
      __syncthreads();
    }
  }
}

__device__ __forceinline__ void gemm_role(int gb, char* smem) {
  char* ws = getp().ws;
  rsrc_t rW = __builtin_amdgcn_make_buffer_rsrc(ws, 0, WS_END, 0x00020000);
  {
    int n = gb >> 1, h = n & 31;
    const FP p = getp();
    gtile<2, 3, 2, E_BF16>((const u16*)(ws + WS_XN), D_, n < 32 ? p.Wk : p.Wv, nullptr, HE_, 10, (gb & 1) * 128, h * E_, rW, n < 32 ? WS_KX : WS_VX, HE_, nullptr, nullptr, smem);
    signal_flag(ws, WS_FQKV + (u32)n * 64);
  }
#pragma unroll 1
  for (int a = gb + 64; a < 768; a += 128) {
    int mb = a / 96, n = a % 96; int mat = n >> 5, h = n & 31;
    const FP p = getp();
    const u16* W = mat == 0 ? p.Wk : (mat == 1 ? p.Wv : p.Wq);
    u32 coff = mat == 0 ? WS_KX : (mat == 1 ? WS_VX : WS_QX);
    gtile<4, 3, 1, E_BF16>((const u16*)(ws + WS_XN), D_, W, W, HE_, 10, mb * 256, h * E_, rW, coff, HE_, nullptr, nullptr, smem);
    signal_flag(ws, WS_FQKV + (u32)(mb * 96 + n) * 64);
  }
#pragma unroll 1
  for (int mb = 0; mb < 7; ++mb) {
    phase_b<false>(mb, gb, ws, rW, smem);
  }
}

constexpr int MST = 656, QST = 80, VST = 208, PST = 72;
constexpr int CL_MEM = 0;
constexpr int CL_Q   = 8 * MST * 2;
constexpr int KBUF   = 48 * QST * 2;
constexpr int VBUF   = 40 * VST * 2;
constexpr int CL_K   = CL_Q + 8 * QST * 2;
constexpr int CL_V   = CL_K + 2 * KBUF;
constexpr int CL_P   = CL_V + 2 * VBUF;
constexpr int CL_F   = CL_P + 8 * PST * 2;
constexpr int CL_END = CL_F + 16;
constexpr int CL_STG = CL_Q;

constexpr int DLY_A2I = 0;
constexpr int DLY_S = 8;
constexpr int DLY_B = 4;
constexpr int DLY_V = 3;
constexpr int DLY_A1 = 0;
constexpr int DLY_A2 = 0;
constexpr int NPOLL = 11;

__device__ __forceinline__ void chain_role(int cu, char* smem) {
  const FP p = getp();
  const int h = cu >> 2, j = cu & 3;
  const int tid = threadIdx.x, lane = tid & 63;
  const int wave = __builtin_amdgcn_readfirstlane(tid >> 6);
  const int fr = lane & 15, g = lane >> 4, q4 = fr >> 2, p4 = fr & 3;
  char* ws = p.ws;
  u32* ctl = (u32*)(ws + WS_CTL);
  rsrc_t rW = __builtin_amdgcn_make_buffer_rsrc(ws, 0, WS_END, 0x00020000);
  const u32 a2base0 = WS_GA2 + (u32)(cu % A2_REP) * A2_STRIDE;

  bf16x8 Wf[20];
  bf16x8 Wof[6];
  {
#pragma unroll 1
    for (int rnd = 0; rnd < 2; ++rnd) {
      if (rnd) __syncthreads();
#pragma unroll 1
      for (int ii = wave; ii < 120; ii += 12) {
        int m2 = ii / 60, c = (ii % 60) * 64 + lane;
        const u16* sp;
        if (rnd == 1 && m2 == 1) { int e = c / 20, ch = c % 20; sp = p.Wo + (size_t)(h * E_ + e) * D_ + j * 160 + ch * 8; }
        else { const u16* W = rnd == 0 ? (m2 == 0 ? p.Wq : p.Wk) : p.Wv; int d = c / 6, ch = c % 6; sp = W + (size_t)d * HE_ + h * E_ + j * 48 + ch * 8; }
        __builtin_amdgcn_global_load_lds((const u32*)sp, (LAS u32*)(smem + ii * 1024), 16, 0, 0);
      }
      asm volatile("s_waitcnt vmcnt(0)" ::: "memory");
      __syncthreads();
      const int mat = wave / 3;
      if ((rnd == 0 && mat < 2) || (rnd == 1 && mat == 2)) {
        const u16* stg = (const u16*)(smem + (rnd == 0 ? mat : 0) * 61440);
        int e0 = (wave % 3) * 16;
#pragma unroll
        for (int ks = 0; ks < 20; ++ks) {
          int r0 = ks * 32 + g * 8 + q4;
          Wf[ks] = cat8(tr_read(stg + r0 * 48 + e0 + p4 * 4), tr_read(stg + (r0 + 4) * 48 + e0 + p4 * 4));
        }
      }
    }
    const u16* stg = (const u16*)(smem + 61440);
    const int wc = wave < 10 ? wave : 0;
#pragma unroll
    for (int t = 0; t < 6; ++t) {
      int r0 = t * 32 + g * 8 + q4;
      Wof[t] = cat8(tr_read(stg + r0 * 160 + wc * 16 + p4 * 4), tr_read(stg + (r0 + 4) * 160 + wc * 16 + p4 * 4));
    }
    __syncthreads();
  }
  for (int i = tid; i < (CL_END - CL_Q) / 4; i += NTH) ((u32*)(smem + CL_Q))[i] = 0u;
  __syncthreads();

  u16* memL = (u16*)(smem + CL_MEM);
  u16* qL = (u16*)(smem + CL_Q);
  u16* pL = (u16*)(smem + CL_P);
  volatile LAS u32* vflag = (volatile LAS u32*)(smem + CL_F);
  volatile LAS u32* pflag = vflag + 1;
  volatile LAS u32* rflag = vflag + 2;
  bool dead = false;

  auto xfetch = [&](int seg, int bf, int ii, int ln) {
    u32 off;
    if (ii < 5) { int c = ii * 64 + ln; int tok = c / 10, ch = c % 10;
      off = ch < 6 ? WS_KX + (u32)((seg * SEG_ + tok) * HE_ + h * E_ + j * 48 + ch * 8) * 2 : 2048u;
      __builtin_amdgcn_global_load_lds((const u32*)(ws + off), (LAS u32*)(smem + CL_K + bf * KBUF + 8 * QST * 2 + ii * 1024), 16, 0, 16);
    } else { int c = (ii - 5) * 64 + ln; int tok = c / 26, ch = c % 26; if (ch >= 24) ch = 0;
      off = WS_VX + (u32)((seg * SEG_ + tok) * HE_ + h * E_ + ch * 8) * 2;
      __builtin_amdgcn_global_load_lds((const u32*)(ws + off), (LAS u32*)(smem + CL_V + bf * VBUF + 8 * VST * 2 + (ii - 5) * 1024), 16, 0, 16);
    }
  };
  auto wait_kv = [&](int mb) {
    u32* fk = (u32*)(ws + WS_FQKV + (u32)(mb * 96 + h) * 64); u32* fv = (u32*)(ws + WS_FQKV + (u32)(mb * 96 + 32 + h) * 64);
    u32 spins = 0;
    for (;;) {
      u32 a = __hip_atomic_load(fk, RLX_AGENT), b = __hip_atomic_load(fv, RLX_AGENT);
      const u32 need = mb == 0 ? 2u : 1u;
      if (__builtin_amdgcn_readfirstlane(a) >= need && __builtin_amdgcn_readfirstlane(b) >= need) break;
      __builtin_amdgcn_s_sleep(4);
      if (dead || spin_fail(spins, ctl)) { dead = true; break; }
    }
    asm volatile("" ::: "memory");
  };
  wait_kv(0);
  for (int ii = wave; ii < 18; ii += 12) xfetch(0, 0, ii, lane);
  asm volatile("s_waitcnt vmcnt(0)" ::: "memory");
  __syncthreads();
  u32 vwx[4];
  auto calc_vwx = [&](int bf, int fr_, int g_) {
    const u16* vb = (const u16*)(smem + CL_V + bf * VBUF) + (8 + fr_) * VST + g_ * 8;
    f32x4 a0 = {0.f, 0.f, 0.f, 0.f}, a1 = {0.f, 0.f, 0.f, 0.f};
#pragma unroll
    for (int t = 0; t < 6; ++t) {
      a0 = mfma16(*(const bf16x8*)(vb + t * 32), Wof[t], a0);
      a1 = mfma16(*(const bf16x8*)(vb + 16 * VST + t * 32), Wof[t], a1);
    }
    vwx[0] = pack2(a0[0], a0[1]); vwx[1] = pack2(a0[2], a0[3]); vwx[2] = pack2(a1[0], a1[1]); vwx[3] = pack2(a1[2], a1[3]);
  };
  calc_vwx(0, fr, g);

#pragma unroll 1
  for (int s = 0; s < NSEG_; ++s) {
    int tz = threadIdx.x; asm volatile("" : "+v"(tz));
    const int tid = tz, lane = tz & 63, fr = lane & 15, g = lane >> 4;
    const u32 ep = (u32)s + 1u;
    const u32 par = ((u32)(s / RNG) + 1u) & 1u;
    const u32 WS_GB = WS_GB0 + (u32)(s % RNG) * GB_SZ, WS_GA1 = WS_GA10 + (u32)(s % RNG) * A1_SZ;
    const u32 a2w = (u32)(s % RNG) * A2_SZ, a2base = a2base0 + (u32)((s + RNG - 1) % RNG) * A2_SZ;
    const int buf = s & 1;
    u16* kL = (u16*)(smem + CL_K + buf * KBUF);
    u16* vL = (u16*)(smem + CL_V + buf * VBUF);
    if (s == 0) {
      for (int c = tid; c < 8 * 80; c += NTH) { int r = c / 80, ch = c % 80; *(u32x4*)(memL + r * MST + ch * 8) = *(const u32x4*)(getp().mem0 + r * D_ + ch * 8); }
    } else if (wave < 10) {
      const u32 epm = (u32)s;
      u32x4 a0, a1; u32 spins = 0;
      for (int i = 0; i < DLY_A2I; ++i) __builtin_amdgcn_s_sleep(4);
      for (;;) {
        asm volatile("" ::: "memory");
        a0 = ld_sc1_16(rW, a2base + tid * 16); a1 = ld_sc1_16(rW, a2base + (tid + 640) * 16);
        bool ok = a0[1] == epm && a0[3] == epm && a1[1] == epm && a1[3] == epm;
        if (__all(ok)) break;
        for (int i = 0; i < DLY_A2; ++i) __builtin_amdgcn_s_sleep(2);
        if (dead || spin_fail(spins, ctl)) { dead = true; break; }
      }
      { int u = tid; int row = ((u >> 4) & 1) * 4 + ((u >> 2) & 3), col = (u >> 5) * 16 + (u & 3) * 4; *(u32x2*)(memL + row * MST + col) = (u32x2){a0[0], a0[2]}; }
      { int u = tid + 640; int row = ((u >> 4) & 1) * 4 + ((u >> 2) & 3), col = (u >> 5) * 16 + (u & 3) * 4; *(u32x2*)(memL + row * MST + col) = (u32x2){a1[0], a1[2]}; }
    }
    lbar();
    if (wave < 9) {
      f32x4 acc = {0.f, 0.f, 0.f, 0.f};
      const u16* mb = memL + (fr & 7) * MST + g * 8;
      bf16x8 b0[4], b1[4];
#pragma unroll
      for (int i = 0; i < 4; ++i) b0[i] = *(const bf16x8*)(mb + i * 32);
#pragma unroll
      for (int gq = 0; gq < 5; ++gq) {
        if (gq & 1) {
          if (gq < 4) {
#pragma unroll
            for (int i = 0; i < 4; ++i) b0[i] = *(const bf16x8*)(mb + ((gq + 1) * 4 + i) * 32);
          }
#pragma unroll
          for (int i = 0; i < 4; ++i) acc = mfma16(Wf[gq * 4 + i], b1[i], acc);
        } else {
          if (gq < 4) {
#pragma unroll
            for (int i = 0; i < 4; ++i) b1[i] = *(const bf16x8*)(mb + ((gq + 1) * 4 + i) * 32);
          }
#pragma unroll
          for (int i = 0; i < 4; ++i) acc = mfma16(Wf[gq * 4 + i], b0[i], acc);
        }
        __builtin_amdgcn_sched_barrier(0);
      }
      u32x2 w = {pack2(acc[0], acc[1]), pack2(acc[2], acc[3])};
      int e = (wave % 3) * 16 + g * 4;
      if (fr < 8) {
        if (wave < 3) *(u32x2*)(qL + fr * QST + e) = w;
        else if (wave < 6) {
          *(u32x2*)(kL + fr * QST + e) = w;
          st_sc1_8(rW, WS_KM + (u32)((s * M_ + fr) * HE_ + h * E_ + j * 48 + e) * 2, w);
        } else {
          st_sc1_16(rW, WS_GB + (u32)(cu * 192 + 96 + (wave - 6) * 32 + fr * 4 + g) * 16, (u32x4){w[0], ep, w[1], ep});
          st_sc1_8(rW, WS_VM + (u32)((s * M_ + fr) * HE_ + h * E_ + j * 48 + e) * 2, w);
        }
      }
    }
    if (s == NSEG_ - 1) break;
    lbar();
    if (wave == 10) {
      if (((s + 1) & 7) == 0) wait_kv((s + 1) >> 3);
#pragma unroll 1
      for (int ii = 0; ii < 18; ++ii) xfetch(s + 1, buf ^ 1, ii, lane);
    }

    if (wave < 3) {
      f32x4 acc = {0.f, 0.f, 0.f, 0.f};
#pragma unroll
      for (int kk = 0; kk < 2; ++kk) {
        bf16x8 a = *(const bf16x8*)(kL + (wave * 16 + fr) * QST + kk * 32 + g * 8);
        bf16x8 b = *(const bf16x8*)(qL + (fr & 7) * QST + kk * 32 + g * 8);
        acc = mfma16(a, b, acc);
      }
      if (fr < 8)
        st_sc1_16(rW, WS_GB + (u32)(cu * 192 + wave * 32 + fr * 4 + g) * 16,
                  (u32x4){(__float_as_uint(acc[0]) & ~1u) | par, (__float_as_uint(acc[1]) & ~1u) | par,
                          (__float_as_uint(acc[2]) & ~1u) | par, (__float_as_uint(acc[3]) & ~1u) | par});
    }
    if (wave >= NPOLL) {
      const int r = lane & 7, q = lane >> 3;
      bool got = true;
      for (int i = 0; i < DLY_B; ++i) __builtin_amdgcn_s_sleep(4);
      const u32 o0 = WS_GB + (u32)(h * 4 * 192 + (q >> 2) * 32 + r * 4 + (q & 3)) * 16;
      const u32 o1 = WS_GB + (u32)(h * 4 * 192 + 64 + r * 4 + (q & 1)) * 16;
      u32x4 a[4], b[4]; u32 spins = 0;
      for (;;) {
        asm volatile("" ::: "memory");
        u32 aa = 0xffffffffu, oo = 0u;
#pragma unroll
        for (int i = 0; i < 4; ++i) {
          a[i] = __builtin_amdgcn_raw_buffer_load_b128(rW, o0, i * 3072, 16);
          b[i] = __builtin_amdgcn_raw_buffer_load_b128(rW, o1, i * 3072, 16);
        }
#pragma unroll
        for (int i = 0; i < 4; ++i) {
          aa &= a[i][0] & a[i][1] & a[i][2] & a[i][3] & b[i][0] & b[i][1] & b[i][2] & b[i][3];
          oo |= a[i][0] | a[i][1] | a[i][2] | a[i][3] | b[i][0] | b[i][1] | b[i][2] | b[i][3];
        }
        bool ok = (aa & 1u) == par && (oo & 1u) == par;
        if (__all(ok)) break;
        if (*pflag == ep) { got = false; break; }
        if (dead || spin_fail(spins, ctl)) { dead = true; break; }
      }
      if (got) {
      const float scale = 0.07216878364870322f;
      float sc[8];
#pragma unroll
      for (int k = 0; k < 4; ++k) {
        float x = (__uint_as_float(a[0][k]) + __uint_as_float(a[1][k])) + (__uint_as_float(a[2][k]) + __uint_as_float(a[3][k]));
        float y = (__uint_as_float(b[0][k]) + __uint_as_float(b[1][k])) + (__uint_as_float(b[2][k]) + __uint_as_float(b[3][k]));
        sc[k] = rbf(rbf(x) * scale); sc[4 + k] = rbf(rbf(y) * scale);
      }
      float mx = fmaxf(fmaxf(fmaxf(sc[0], sc[1]), fmaxf(sc[2], sc[3])), fmaxf(fmaxf(sc[4], sc[5]), fmaxf(sc[6], sc[7])));
      mx = fmaxf(mx, __uint_as_float(__builtin_amdgcn_update_dpp(0, __float_as_uint(mx), 0x128, 0xf, 0xf, false)));
      mx = red16_max(mx); mx = red32_max(mx);
      float sum = 0.f, sum2 = 0.f;
#pragma unroll
      for (int k = 0; k < 4; ++k) { sc[k] = __expf(sc[k] - mx); sum += sc[k]; sc[4 + k] = __expf(sc[4 + k] - mx); sum2 += sc[4 + k]; }
      if (q < 2) sum += sum2;
      sum += __uint_as_float(__builtin_amdgcn_update_dpp(0, __float_as_uint(sum), 0x128, 0xf, 0xf, false));
      sum = red16_add(sum); sum = red32_add(sum);
      float inv = __builtin_amdgcn_rcpf(sum);
      *(u32x2*)(pL + r * PST + q * 4) = (u32x2){pack2(sc[0] * inv, sc[1] * inv), pack2(sc[2] * inv, sc[3] * inv)};
      if (q < 2) *(u32x2*)(pL + r * PST + 32 + q * 4) = (u32x2){pack2(sc[4] * inv, sc[5] * inv), pack2(sc[6] * inv, sc[7] * inv)};
      asm volatile("s_waitcnt lgkmcnt(0)" ::: "memory");
      if (lane == 0) *pflag = ep;
      }
    } else if (wave == 9) {
      u32x4 v[6]; u32 spins = 0;
      for (int i = 0; i < DLY_V; ++i) __builtin_amdgcn_s_sleep(4);
      for (;;) {
        asm volatile("" ::: "memory");
        bool ok = true;
#pragma unroll
        for (int i = 0; i < 6; ++i) { int id = lane + 64 * i; int src = id / 96, u = id % 96; v[i] = ld_sc1_16(rW, WS_GB + (u32)((h * 4 + src) * 192 + 96 + u) * 16); }
#pragma unroll
        for (int i = 0; i < 6; ++i) ok = ok && v[i][1] == ep && v[i][3] == ep;
        if (__all(ok)) break;
        if (dead || spin_fail(spins, ctl)) { dead = true; break; }
      }
#pragma unroll
      for (int i = 0; i < 6; ++i) {
        int id = lane + 64 * i; int src = id / 96, u = id % 96; int vt = u >> 5, rr = (u >> 2) & 7, gg = u & 3;
        *(u32x2*)(vL + rr * VST + src * 48 + vt * 16 + gg * 4) = (u32x2){v[i][0], v[i][2]};
      }
      asm volatile("s_waitcnt lgkmcnt(0)" ::: "memory");
      if (lane == 0) *vflag = ep;
    }
    if (wave < 10) {
      while (*vflag != ep) { if (dead) break; }
      asm volatile("" ::: "memory");
      f32x4 am = {0.f, 0.f, 0.f, 0.f};
      const u16* vb = vL + (fr & 7) * VST + g * 8;
#pragma unroll
      for (int t = 0; t < 6; ++t) am = mfma16(*(const bf16x8*)(vb + t * 32), Wof[t], am);
      u32x4 a0u = {vwx[0], vwx[1], vwx[2], vwx[3]};
      u32x4 a1u = {g < 2 ? pack2(am[0], am[1]) : 0u, g < 2 ? pack2(am[2], am[3]) : 0u, 0u, 0u};
      while (*pflag != ep) { if (dead) break; }
      asm volatile("s_waitcnt vmcnt(0)" ::: "memory");
      const u16* pr = pL + (fr & 7) * PST;
      u32x2 x0 = *(const u32x2*)(pr + 8 + g * 4), x1 = *(const u32x2*)(pr + 24 + g * 4), x2 = *(const u32x2*)(pr + (g & 1) * 4);
      u32x4 b0u = {x0[0], x0[1], x1[0], x1[1]};
      u32x4 b1u = {g < 2 ? x2[0] : 0u, g < 2 ? x2[1] : 0u, 0u, 0u};
      f32x4 acc = {0.f, 0.f, 0.f, 0.f};
      acc = mfma16(__builtin_bit_cast(bf16x8, a0u), __builtin_bit_cast(bf16x8, b0u), acc);
      acc = mfma16(__builtin_bit_cast(bf16x8, a1u), __builtin_bit_cast(bf16x8, b1u), acc);
      if (fr < 8) {
        u32 o = WS_GA1 + (u32)(((j * 10 + wave) * 32 + h) * 32 + fr * 4 + g) * 16;
        st_sc1_16(rW, o, (u32x4){(__float_as_uint(acc[0]) & ~1u) | par, (__float_as_uint(acc[1]) & ~1u) | par,
                                 (__float_as_uint(acc[2]) & ~1u) | par, (__float_as_uint(acc[3]) & ~1u) | par});
      }
    }
    if (wave == 10) asm volatile("s_waitcnt vmcnt(0)" ::: "memory");
    lbar();
    if ((s & 7) == 7 && tid == 0) __hip_atomic_store((u32*)(ws + WS_FCH + (u32)cu * 64), (u32)(s >> 3) + 1u, RLX_AGENT);
    if (wave < 10) calc_vwx(buf ^ 1, fr, g);
    if (wave >= NPOLL) {
      if (cu < 80) {
        const int ch = lane & 15, hq = lane >> 4;
        bool got = true;
        for (int i = 0; i < DLY_A1; ++i) __builtin_amdgcn_s_sleep(4);
        const u32 rbase = WS_GA1 + (u32)(((cu >> 1) * 32 + hq * 8) * 32 + (cu & 1) * 16 + ch) * 16;
        u32x4 a[8]; u32 spins = 0;
        for (;;) {
          asm volatile("" ::: "memory");
          u32 aa = 0xffffffffu, oo = 0u;
#pragma unroll
          for (int i = 0; i < 8; ++i) a[i] = __builtin_amdgcn_raw_buffer_load_b128(rW, rbase, i * 512, 16);
#pragma unroll
          for (int i = 0; i < 8; ++i) { aa &= a[i][0] & a[i][1] & a[i][2] & a[i][3]; oo |= a[i][0] | a[i][1] | a[i][2] | a[i][3]; }
          bool ok = (aa & 1u) == par && (oo & 1u) == par;
          if (__all(ok)) break;
          if (*rflag == ep) { got = false; break; }
          if (dead || spin_fail(spins, ctl)) { dead = true; break; }
        }
        if (got) {
        float t[4];
#pragma unroll
        for (int k = 0; k < 4; ++k) {
          float x = ((__uint_as_float(a[0][k]) + __uint_as_float(a[1][k])) + (__uint_as_float(a[2][k]) + __uint_as_float(a[3][k])))
                  + ((__uint_as_float(a[4][k]) + __uint_as_float(a[5][k])) + (__uint_as_float(a[6][k]) + __uint_as_float(a[7][k])));
          x = red16_add(x); x = red32_add(x); t[k] = x;
        }
        u32 w0 = pack2(t[0], t[1]), w1 = pack2(t[2], t[3]);
#pragma unroll
        for (int it = 0; it < A2_REP / 4; ++it) st_sc1_16(rW, a2w + WS_GA2 + (u32)(cu * 16 + ch) * 16 + (u32)(hq + 4 * it) * A2_STRIDE, (u32x4){w0, ep, w1, ep});
        if (lane == 0) *rflag = ep;
        }
      }
      else if (wave == 11) {
        u32 spins = 0;
        for (int i = 0; i < DLY_S; ++i) __builtin_amdgcn_s_sleep(4);
        for (;;) {
          asm volatile("" ::: "memory");
          u32x4 a = ld_sc1_16(rW, a2base0 + a2w + (u32)(cu - 80) * 256);
          if (__all(a[1] == ep)) break;
          if (dead || spin_fail(spins, ctl)) { dead = true; break; }
        }
      }
    }
    lbar();
  }
  asm volatile("s_waitcnt vmcnt(0)" ::: "memory");
  __syncthreads();
  if (tid == 0) __hip_atomic_store((u32*)(ws + WS_FCH + (u32)cu * 64), 8u, RLX_AGENT);
  phase_b<true>(7, cu, ws, rW, smem);
}

__global__ __launch_bounds__(NTH, 3) void k_main(FP p) {
  extern __shared__ __attribute__((aligned(16))) char smem[];
  const int b = blockIdx.x;
  const int id = (b >> 4) * 8 + (b & 7);
  if ((b >> 3) & 1) gemm_role(id, smem);
  else chain_role(id, smem);
}

extern "C" void kernel_launch(void* const* d_in, const int* in_sizes, int n_in,
                              void* d_out, int out_size, void* d_ws, size_t ws_size,
                              hipStream_t stream) {
  char* ws = (char*)d_ws;
  (void)hipFuncSetAttribute((const void*)k_main, hipFuncAttributeMaxDynamicSharedMemorySize, LDS_BYTES);
  k_ln<<<S_ / 4, 256, 0, stream>>>((const u16*)d_in[0], (u16*)(ws + WS_XN), (u32x4*)ws,
                                  (const u32x4*)d_in[2], (const u32x4*)d_in[3], (const u32x4*)d_in[4], (const u32x4*)d_in[5]);
  FP p{(const u16*)d_in[0], (const u16*)d_in[1], (const u16*)d_in[2], (const u16*)d_in[3], (const u16*)d_in[4], (const u16*)d_in[5],
       (const u16*)d_in[8], (const u16*)d_in[9], (const u16*)d_in[10], (u16*)d_out, ws};
  k_main<<<256, NTH, LDS_BYTES, stream>>>(p);
}
```

```cpp
#include <hip/hip_runtime.h>
#include <hip/hip_bf16.h>
#include <stdint.h>

typedef unsigned short u16;
typedef unsigned int u32;
typedef unsigned long long u64;
typedef short bf16x8 __attribute__((ext_vector_type(8)));
typedef short bf16x4 __attribute__((ext_vector_type(4)));
typedef float f32x4 __attribute__((ext_vector_type(4)));
typedef u32 u32x4 __attribute__((ext_vector_type(4)));
typedef u32 u32x2 __attribute__((ext_vector_type(2)));
#define LAS __attribute__((address_space(3)))
#define RLX_AGENT __ATOMIC_RELAXED, __HIP_MEMORY_SCOPE_AGENT

constexpr int S_ = 2048, D_ = 640, H_ = 32, E_ = 192, HE_ = H_ * E_;
constexpr int M_ = 8, SEG_ = 32, NSEG_ = 64, F_ = 1536;

constexpr u32 WS_CTL   = 0;
constexpr u32 WS_FQKV  = 4096;
constexpr u32 WS_FCH   = WS_FQKV + 8 * 96 * 64;
constexpr u32 WS_FAT   = WS_FCH + 128 * 64;
constexpr u32 WS_FOP   = WS_FAT + 64 * 64;
constexpr u32 WS_FLN   = WS_FOP + 32 * 64;
constexpr u32 WS_FGL   = WS_FLN + 32 * 64;
constexpr u32 WS_GA2   = (WS_FGL + 32 * 64 + 4095) / 4096 * 4096;
constexpr int A2R = 8;
constexpr int A2_REP = A2R, A2_STRIDE = 20736;
constexpr int RNG = 8;
constexpr u32 A2_SZ = A2_REP * A2_STRIDE, GB_SZ = 128 * 3072, A1_SZ = 40 * 32 * 512;
constexpr u32 WS_GB0   = WS_GA2 + RNG * A2_SZ;
constexpr u32 WS_GA10  = WS_GB0 + RNG * GB_SZ;
constexpr u32 WS_ZEND  = WS_GA10 + RNG * A1_SZ;
constexpr u32 WS_XN    = (WS_ZEND + 4095) / 4096 * 4096;
constexpr u32 WS_KX    = WS_XN + S_ * D_ * 2;
constexpr u32 WS_VX    = WS_KX + S_ * HE_ * 2;
constexpr u32 WS_QX    = WS_VX + S_ * HE_ * 2;
constexpr u32 WS_KM    = WS_QX + S_ * HE_ * 2;
constexpr u32 WS_VM    = WS_KM + NSEG_ * M_ * HE_ * 2;
constexpr u32 WS_OX    = WS_VM + NSEG_ * M_ * HE_ * 2;
constexpr u32 WS_H     = WS_OX + S_ * HE_ * 2;
constexpr u32 WS_HN    = WS_H + S_ * D_ * 2;
constexpr u32 WS_GU    = WS_HN + S_ * D_ * 2;
constexpr u32 WS_SLAB  = WS_GU + S_ * F_ * 2;
constexpr int NSLAB = 12;
constexpr u32 WS_END   = WS_SLAB + (u32)NSLAB * S_ * D_ * 4;

__device__ __forceinline__ float bf2f(u16 v) { return __uint_as_float(((u32)v) << 16); }
__device__ __forceinline__ u16 f2bf(float f) {
  __hip_bfloat16 b = __float2bfloat16(f);
  return *reinterpret_cast<u16*>(&b);
}
__device__ __forceinline__ float rbf(float f) { return bf2f(f2bf(f)); }
__device__ __forceinline__ u32 pack2(float a, float b) { return (u32)f2bf(a) | ((u32)f2bf(b) << 16); }
__device__ __forceinline__ f32x4 mfma16(bf16x8 a, bf16x8 b, f32x4 c) {
  return __builtin_amdgcn_mfma_f32_16x16x32_bf16(a, b, c, 0, 0, 0);
}
__device__ __forceinline__ bf16x4 tr_read(const void* p) {
  return __builtin_amdgcn_ds_read_tr16_b64_v4i16((LAS bf16x4*)p);
}
__device__ __forceinline__ bf16x8 cat8(bf16x4 a, bf16x4 b) {
  bf16x8 r; r[0]=a[0]; r[1]=a[1]; r[2]=a[2]; r[3]=a[3]; r[4]=b[0]; r[5]=b[1]; r[6]=b[2]; r[7]=b[3]; return r;
}
__device__ __forceinline__ void lbar() { asm volatile("s_waitcnt lgkmcnt(0)\n\ts_barrier" ::: "memory"); }
typedef __amdgpu_buffer_rsrc_t rsrc_t;
__device__ __forceinline__ u32x4 ld_sc1_16(rsrc_t r, u32 off) { return __builtin_amdgcn_raw_buffer_load_b128(r, off, 0, 16); }
__device__ __forceinline__ void st_sc1_16(rsrc_t r, u32 off, u32x4 v) { __builtin_amdgcn_raw_buffer_store_b128(v, r, off, 0, 16); }
__device__ __forceinline__ void st_sc1_8(rsrc_t r, u32 off, u32x2 v) { __builtin_amdgcn_raw_buffer_store_b64(v, r, off, 0, 16); }
__device__ __forceinline__ float red16_max(float x) {
  auto r = __builtin_amdgcn_permlane16_swap(__float_as_uint(x), __float_as_uint(x), false, false);
  return fmaxf(__uint_as_float(r[0]), __uint_as_float(r[1]));
}
__device__ __forceinline__ float red32_max(float x) {
  auto r = __builtin_amdgcn_permlane32_swap(__float_as_uint(x), __float_as_uint(x), false, false);
  return fmaxf(__uint_as_float(r[0]), __uint_as_float(r[1]));
}
__device__ __forceinline__ float red16_add(float x) {
  auto r = __builtin_amdgcn_permlane16_swap(__float_as_uint(x), __float_as_uint(x), false, false);
  return __uint_as_float(r[0]) + __uint_as_float(r[1]);
}
__device__ __forceinline__ float red32_add(float x) {
  auto r = __builtin_amdgcn_permlane32_swap(__float_as_uint(x), __float_as_uint(x), false, false);
  return __uint_as_float(r[0]) + __uint_as_float(r[1]);
}

constexpr u32 SPIN_CAP = 1u << 18;
__device__ __forceinline__ bool spin_fail(u32& spins, u32* ctl) {
  ++spins;
  if (spins > SPIN_CAP) { __hip_atomic_store(ctl, 1u, RLX_AGENT); return true; }
  return (spins & 255u) == 0u && __builtin_amdgcn_readfirstlane(__hip_atomic_load(ctl, RLX_AGENT)) != 0u;
}

struct FP {
  const u16* x; const u16* mem0; const u16* Wq; const u16* Wk; const u16* Wv; const u16* Wo;
  const u16* wg; const u16* wu; const u16* wd; u16* out; char* ws;
};
typedef const __attribute__((address_space(4))) FP* kargp_t;
__device__ __forceinline__ FP getp() {
#if defined(__HIP_DEVICE_COMPILE__)
  kargp_t kp = (kargp_t)__builtin_amdgcn_kernarg_segment_ptr(); asm volatile("" : "+s"(kp)); return *kp;
#else
  return FP{};
#endif
}
constexpr int NTH = 768;
constexpr int LDS_BYTES = 153600;

__device__ __forceinline__ void ln_row(const float (&v)[10], u32 (&o)[5]) {
  float sum = 0.f;
#pragma unroll
  for (int i = 0; i < 10; ++i) sum += v[i];
#pragma unroll
  for (int m = 32; m >= 1; m >>= 1) sum += __shfl_xor(sum, m);
  float mean = sum * (1.0f / D_);
  float sq = 0.f;
#pragma unroll
  for (int i = 0; i < 10; ++i) { float d = v[i] - mean; sq += d * d; }
#pragma unroll
  for (int m = 32; m >= 1; m >>= 1) sq += __shfl_xor(sq, m);
  float var = sq * (1.0f / D_);
  float mb = rbf(mean), vb = rbf(var);
  float den = rbf(sqrtf(rbf(vb + 1e-5f)));
#pragma unroll
  for (int i = 0; i < 5; ++i) o[i] = pack2(rbf(v[2 * i] - mb) / den, rbf(v[2 * i + 1] - mb) / den);
}
__global__ __launch_bounds__(256) void k_ln(const u16* __restrict__ x, u16* __restrict__ y, u32x4* __restrict__ zero) {
  for (u32 i = blockIdx.x * 256 + threadIdx.x; i < WS_ZEND / 16; i += (S_ / 4) * 256) zero[i] = (u32x4){0u, 0u, 0u, 0u};
  int row = blockIdx.x * 4 + (threadIdx.x >> 6);
  int lane = threadIdx.x & 63;
  const u32* xr = (const u32*)(x + (size_t)row * D_);
  float v[10];
#pragma unroll
  for (int i = 0; i < 5; ++i) { u32 w = xr[i * 64 + lane]; v[2 * i] = bf2f((u16)(w & 0xffff)); v[2 * i + 1] = bf2f((u16)(w >> 16)); }
  u32 o[5]; ln_row(v, o);
  u32* yr = (u32*)(y + (size_t)row * D_);
#pragma unroll
  for (int i = 0; i < 5; ++i) yr[i * 64 + lane] = o[i];
}

__device__ __forceinline__ void wait_flag(char* ws, u32 foff, u32 target) {
  if (threadIdx.x == 0) {
    u32* f = (u32*)(ws + foff); u32* ctl = (u32*)(ws + WS_CTL); u32 spins = 0;
    while (__hip_atomic_load(f, RLX_AGENT) < target) {
      __builtin_amdgcn_s_sleep(8);
      if (++spins > (1u << 20)) { __hip_atomic_store(ctl, 1u, RLX_AGENT); break; }
      if ((spins & 63u) == 0u && __hip_atomic_load(ctl, RLX_AGENT) != 0u) break;
    }
  }
}
__device__ __forceinline__ void acquire_bar() {
  if (threadIdx.x == 0) { __builtin_amdgcn_fence(__ATOMIC_ACQUIRE, "agent"); asm volatile("s_waitcnt vmcnt(0)" ::: "memory"); }
  __syncthreads();
}
__device__ __forceinline__ void signal_flag(char* ws, u32 foff) {
  asm volatile("s_waitcnt vmcnt(0)" ::: "memory");
  __syncthreads();
  if (threadIdx.x == 0) __hip_atomic_fetch_add((u32*)(ws + foff), 1u, RLX_AGENT);
}

template <int NWN> __device__ __forceinline__ int bswz(int row) {
  if (NWN == 3) return (((row >> 1) & 1) | (((row >> 3) & 1) << 1)) << 1;
  else if (NWN == 2) return ((row & 3) << 2) | ((row >> 2) & 3);
  else return (((row >> 1) & 1) | (((row >> 3) & 1) << 1)) << 1;
}
enum { E_BF16 = 0, E_SLAB = 1, E_GLU = 2, E_DOWN = 3 };
template <int NWM, int NWN, int KG, int EPI>
__device__ __forceinline__ void gtile(const u16* __restrict__ A, int lda, const u16* __restrict__ B0, const u16* __restrict__ B1, int ldb,
                                      int nk, int m0, int nb, rsrc_t rW, u32 coff, int ldc, const u16* resid, u16* outp, char* smem) {
  constexpr int BM = NWM * 64, BN = NWN * 64, BROW = BN * 2, NA = BM / 16, NI = NA + BN / 16, NT = NI * KG, ST_BYTES = NT * 1024;
  constexpr int NST = (143360 / ST_BYTES) < 16 ? (143360 / ST_BYTES) : 16, PD = NST - 1;
  constexpr int NPW = (NT + 11) / 12;
  constexpr int NW = NWM * NWN;
  int tz = threadIdx.x; asm volatile("" : "+v"(tz));
  const int tid = tz, lane = tid & 63, wave = __builtin_amdgcn_readfirstlane(tid >> 6);
  const int kg = wave / NW, wv = wave % NW, wm = wv % NWM, wn = wv / NWM;
  const int fr = lane & 15, g = lane >> 4;
  const u16* src[NPW]; u32 sstep[NPW];
#pragma unroll
  for (int i = 0; i < NPW; ++i) {
    int it = wave + 12 * i; if (it >= NT) it = 0;
    int sub = it / NI, ii = it % NI;
    if (ii < NA) { int b = ii * 1024 + lane * 16; int row = b >> 6, pos = (b >> 4) & 3, ch = pos ^ ((4 - (row >> 2)) & 3);
      src[i] = A + (size_t)(m0 + row) * lda + ch * 8 + sub * 32; sstep[i] = 32 * KG; }
    else { int b = (ii - NA) * 1024 + lane * 16; int row = b / BROW, pos = (b % BROW) >> 4;
      int ch = pos ^ bswz<NWN>(row);
      if (EPI == E_GLU) { int w2 = ch >> 3, within = ch & 7; const u16* base = (within >> 2) ? B1 : B0;
        src[i] = base + (size_t)(row + sub * 32) * ldb + nb + w2 * 32 + (within & 3) * 8; }
      else src[i] = B0 + (size_t)(row + sub * 32) * ldb + nb + ch * 8;
      sstep[i] = 32u * KG * (u32)ldb; }
  }
  const int npw = (wave + 12 * (NPW - 1)) < NT ? NPW : NPW - 1;
  auto stage = [&](int kt, int buf) {
    char* sb = smem + buf * ST_BYTES;
#pragma unroll
    for (int i = 0; i < NPW; ++i)
      if (i < npw) __builtin_amdgcn_global_load_lds((const u32*)(src[i] + (size_t)kt * sstep[i]), (LAS u32*)(sb + (wave + 12 * i) * 1024), 16, 0, 0);
  };
  f32x4 acc[4][4];
#pragma unroll
  for (int a = 0; a < 4; ++a)
#pragma unroll
    for (int b = 0; b < 4; ++b) acc[a][b] = (f32x4){0.f, 0.f, 0.f, 0.f};
  const bool compk = wave < NW * KG;
  const bool comp = wave < NW;
  const int nst = nk * 2 / KG;
#pragma unroll 1
  for (int i = 0; i < PD; ++i) if (i < nst) stage(i, i);
  int cb = 0, pb = PD;
  const int q = fr >> 2, pp = fr & 3;
  const int r0 = g * 8 + q, r1 = r0 + 4;
  const int x0 = bswz<NWN>(r0), x1 = bswz<NWN>(r1);
  const int aoff = kg * NI * 1024 + (wm * 64 + fr) * 64 + ((g ^ ((4 - (fr >> 2)) & 3)) << 4);
#pragma unroll 1
  for (int kt = 0; kt < nst; ++kt) {
    if (kt + PD <= nst) {
      if (npw == NPW) asm volatile("s_waitcnt vmcnt(%0)" :: "i"(NPW * (PD - 1)) : "memory");
      else asm volatile("s_waitcnt vmcnt(%0)" :: "i"((NPW - 1) * (PD - 1)) : "memory");
    }
    else asm volatile("s_waitcnt vmcnt(0)" ::: "memory");
    lbar();
    if (kt + PD < nst) stage(kt + PD, pb);
    if (compk) {
      const char* sa = smem + cb * ST_BYTES; const char* sb = sa + (kg * NI + NA) * 1024;
      bf16x8 af[4], bfr[4];
#pragma unroll
      for (int a = 0; a < 4; ++a) af[a] = *(const bf16x8*)(sa + aoff + a * 1024);
#pragma unroll
      for (int b = 0; b < 4; ++b) {
        int ch = wn * 8 + b * 2 + (pp >> 1);
        bfr[b] = cat8(tr_read(sb + r0 * BROW + ((ch ^ x0) << 4) + 8 * (pp & 1)), tr_read(sb + r1 * BROW + ((ch ^ x1) << 4) + 8 * (pp & 1)));
      }
#pragma unroll
      for (int a = 0; a < 4; ++a)
#pragma unroll
        for (int b = 0; b < 4; ++b) acc[a][b] = mfma16(bfr[b], af[a], acc[a][b]);
    }
    cb = cb == NST - 1 ? 0 : cb + 1; pb = pb == NST - 1 ? 0 : pb + 1;
  }
  if (KG > 1) {
    lbar();
    if (compk && kg > 0) {
      f32x4* d = (f32x4*)(smem + (wave - NW) * 16384) + lane;
#pragma unroll
      for (int a = 0; a < 4; ++a)
#pragma unroll
        for (int b = 0; b < 4; ++b) d[(a * 4 + b) * 64] = acc[a][b];
    }
    lbar();
    if (comp) {
#pragma unroll
      for (int k2 = 1; k2 < KG; ++k2) {
        const f32x4* d = (const f32x4*)(smem + ((k2 - 1) * NW + wv) * 16384) + lane;
#pragma unroll
        for (int a = 0; a < 4; ++a)
#pragma unroll
          for (int b = 0; b < 4; ++b) acc[a][b] += d[(a * 4 + b) * 64];
      }
    }
  }
  if (EPI == E_DOWN) {
    if (comp) {
#pragma unroll
      for (int a = 0; a < 4; ++a) {
        const int m = m0 + wm * 64 + a * 16 + fr;
#pragma unroll
        for (int b = 0; b < 4; ++b) {
          int n = nb + wn * 64 + b * 16 + g * 4;
          u32x2 rv = *(const u32x2*)(resid + (size_t)m * ldc + n);
          float o0 = rbf(acc[a][b][0]) + bf2f((u16)(rv[0] & 0xffff)), o1 = rbf(acc[a][b][1]) + bf2f((u16)(rv[0] >> 16));
          float o2 = rbf(acc[a][b][2]) + bf2f((u16)(rv[1] & 0xffff)), o3 = rbf(acc[a][b][3]) + bf2f((u16)(rv[1] >> 16));
          *(u32x2*)(outp + (size_t)m * ldc + n) = (u32x2){pack2(o0, o1), pack2(o2, o3)};
        }
      }
    }
  } else {
    constexpr int OC = EPI == E_GLU ? NWN * 32 : BN;
    constexpr int ES = EPI == E_SLAB ? 4 : 2;
    constexpr int RB = OC * ES, RS = RB + 16, CPR = RB / 16;
    lbar();
    if (comp) {
#pragma unroll
      for (int a = 0; a < 4; ++a) {
        char* rowp = smem + (wm * 64 + a * 16 + fr) * RS;
        if (EPI == E_GLU) {
#pragma unroll
          for (int b = 0; b < 2; ++b) {
            float o[4];
#pragma unroll
            for (int r = 0; r < 4; ++r) o[r] = rbf(acc[a][b][r]) * rbf(acc[a][b + 2][r]);
            *(u32x2*)(rowp + (wn * 32 + b * 16 + g * 4) * 2) = (u32x2){pack2(o[0], o[1]), pack2(o[2], o[3])};
          }
        } else {
#pragma unroll
          for (int b = 0; b < 4; ++b) {
            if (EPI == E_BF16) *(u32x2*)(rowp + (wn * 64 + b * 16 + g * 4) * 2) = (u32x2){pack2(acc[a][b][0], acc[a][b][1]), pack2(acc[a][b][2], acc[a][b][3])};
            else *(f32x4*)(rowp + (wn * 64 + b * 16 + g * 4) * 4) = acc[a][b];
          }
        }
      }
    }
    lbar();
#pragma unroll 2
    for (int c = tid; c < BM * CPR; c += NTH) {
      int row = c / CPR, ch = c % CPR;
      u32x4 v = *(const u32x4*)(smem + row * RS + ch * 16);
      st_sc1_16(rW, coff + (u32)((m0 + row) * ldc + nb) * ES + ch * 16, v);
    }
  }
}

__device__ __forceinline__ void attn_wave(const char* ws, rsrc_t rW, int s, int h, u16* vL) {
  int tz = threadIdx.x; asm volatile("" : "+v"(tz));
  const int lane = tz & 63, fr = lane & 15, g = lane >> 4, q4 = fr >> 2, p4 = fr & 3;
  const u16* qx = (const u16*)(ws + WS_QX); const u16* kx = (const u16*)(ws + WS_KX); const u16* vx = (const u16*)(ws + WS_VX);
  const u16* km = (const u16*)(ws + WS_KM); const u16* vm = (const u16*)(ws + WS_VM);
#pragma unroll
  for (int i = 0; i < 16; ++i) {
    int c = i * 64 + lane; int row = c / 25, ch = c % 25; if (row > 39) row = 39; if (ch > 23) ch = 23;
    const u16* sp = row < 8 ? vm + ((size_t)(s * M_ + row)) * HE_ : vx + ((size_t)(s * SEG_ + row - 8)) * HE_;
    __builtin_amdgcn_global_load_lds((const u32*)(sp + h * E_ + ch * 8), (LAS u32*)((char*)vL + i * 1024), 16, 0, 0);
  }
  f32x4 sc[3][2];
#pragma unroll
  for (int a = 0; a < 3; ++a) { sc[a][0] = (f32x4){0.f, 0.f, 0.f, 0.f}; sc[a][1] = (f32x4){0.f, 0.f, 0.f, 0.f}; }
  {
    const u16* kp[3];
#pragma unroll
    for (int a = 0; a < 3; ++a) {
      int key = a * 16 + fr; if (key > 39) key = 39;
      kp[a] = (key < 8 ? km + ((size_t)(s * M_ + key)) * HE_ : kx + ((size_t)(s * SEG_ + key - 8)) * HE_) + h * E_ + g * 8;
    }
    const u16* qp0 = qx + ((size_t)(s * SEG_ + fr)) * HE_ + h * E_ + g * 8;
    const u16* qp1 = qp0 + (size_t)16 * HE_;
#pragma unroll
    for (int hf = 0; hf < 2; ++hf) {
      bf16x8 qf[3][2], kf[3][3];
#pragma unroll
      for (int kk = 0; kk < 3; ++kk) {
        qf[kk][0] = *(const bf16x8*)(qp0 + (hf * 3 + kk) * 32); qf[kk][1] = *(const bf16x8*)(qp1 + (hf * 3 + kk) * 32);
#pragma unroll
        for (int a = 0; a < 3; ++a) kf[kk][a] = *(const bf16x8*)(kp[a] + (hf * 3 + kk) * 32);
      }
#pragma unroll
      for (int kk = 0; kk < 3; ++kk)
#pragma unroll
        for (int a = 0; a < 3; ++a) { sc[a][0] = mfma16(kf[kk][a], qf[kk][0], sc[a][0]); sc[a][1] = mfma16(kf[kk][a], qf[kk][1], sc[a][1]); }
    }
  }
  const float scale = 0.07216878364870322f;
  bf16x8 pb[2][2];
#pragma unroll
  for (int qt = 0; qt < 2; ++qt) {
    int qrow = qt * 16 + fr;
    float v[12]; float mx = -3.0e38f;
#pragma unroll
    for (int a = 0; a < 3; ++a)
#pragma unroll
      for (int r = 0; r < 4; ++r) {
        int key = a * 16 + g * 4 + r;
        float x = rbf(rbf(sc[a][qt][r]) * scale);
        bool vis = key < 8 || (key - 8) <= qrow;
        x = vis ? x : rbf(-1000000000.0f);
        v[a * 4 + r] = x;
        if (key < 40) mx = fmaxf(mx, x);
      }
    mx = red16_max(mx); mx = red32_max(mx);
    float sum = 0.f;
#pragma unroll
    for (int a = 0; a < 3; ++a)
#pragma unroll
      for (int r = 0; r < 4; ++r) {
        int key = a * 16 + g * 4 + r;
        float e = key < 40 ? __expf(v[a * 4 + r] - mx) : 0.f;
        v[a * 4 + r] = e; sum += e;
      }
    sum = red16_add(sum); sum = red32_add(sum);
    float inv = 1.0f / sum;
#pragma unroll
    for (int r = 0; r < 4; ++r) {
      pb[qt][0][r] = (short)f2bf(v[r] * inv); pb[qt][0][4 + r] = (short)f2bf(v[4 + r] * inv);
      pb[qt][1][r] = (short)f2bf(v[8 + r] * inv); pb[qt][1][4 + r] = 0;
    }
  }
  asm volatile("s_waitcnt vmcnt(0)" ::: "memory");
#pragma unroll
  for (int i = 0; i < 4; ++i) { int c = i * 64 + lane; if (c < 200) *(u32x4*)(vL + 40 * 200 + c * 8) = (u32x4){0u, 0u, 0u, 0u}; }
  asm volatile("s_waitcnt lgkmcnt(0)" ::: "memory");
  u32x2 ov[12][2];
#pragma unroll
  for (int et = 0; et < 12; ++et) {
    int r0 = g * 4 + q4;
    bf16x8 a0 = cat8(tr_read(vL + r0 * 200 + et * 16 + p4 * 4), tr_read(vL + (16 + r0) * 200 + et * 16 + p4 * 4));
    bf16x4 t2 = tr_read(vL + (32 + r0) * 200 + et * 16 + p4 * 4);
    bf16x8 a1 = cat8(t2, t2);
#pragma unroll
    for (int qt = 0; qt < 2; ++qt) {
      f32x4 acc = {0.f, 0.f, 0.f, 0.f};
      acc = mfma16(a0, pb[qt][0], acc);
      acc = mfma16(a1, pb[qt][1], acc);
      ov[et][qt] = (u32x2){pack2(acc[0], acc[1]), pack2(acc[2], acc[3])};
    }
  }
  asm volatile("s_waitcnt lgkmcnt(0)" ::: "memory");
#pragma unroll
  for (int et = 0; et < 12; ++et)
#pragma unroll
    for (int qt = 0; qt < 2; ++qt) *(u32x2*)(vL + (qt * 16 + fr) * 200 + et * 16 + g * 4) = ov[et][qt];
  asm volatile("s_waitcnt lgkmcnt(0)" ::: "memory");
#pragma unroll
  for (int i = 0; i < 12; ++i) {
    int c = i * 64 + lane; int row = c / 24, ch = c % 24;
    u32x4 v = *(const u32x4*)(vL + row * 200 + ch * 8);
    st_sc1_16(rW, WS_OX + (u32)((s * SEG_ + row) * HE_ + h * E_ + ch * 8) * 2, v);
  }
}

template <int NS>
__device__ __forceinline__ void redln_row(char* ws, rsrc_t rW, int row) {
  const FP p = getp();
  const int lane = threadIdx.x & 63;
  const bool two = lane < 16;
  float v[16];
#pragma unroll
  for (int hf = 0; hf < 2; ++hf) {
    int c0 = (hf == 0 ? lane : (two ? 64 + lane : lane)) * 8;
    const f32x4* sl = (const f32x4*)((const float*)(ws + WS_SLAB) + (size_t)row * D_ + c0);
    f32x4 lo[NS], hi[NS];
#pragma unroll
    for (int q = 0; q < NS; ++q) { lo[q] = sl[(size_t)q * (S_ * D_ / 4)]; hi[q] = sl[(size_t)q * (S_ * D_ / 4) + 1]; }
    f32x4 s0 = ((lo[0] + lo[1]) + (lo[2] + lo[3])) + ((lo[4] + lo[5]) + (lo[6] + lo[7]));
    f32x4 s1 = ((hi[0] + hi[1]) + (hi[2] + hi[3])) + ((hi[4] + hi[5]) + (hi[6] + hi[7]));
    if (NS == 12) { s0 += (lo[8 % NS] + lo[9 % NS]) + (lo[10 % NS] + lo[11 % NS]); s1 += (hi[8 % NS] + hi[9 % NS]) + (hi[10 % NS] + hi[11 % NS]); }
    u32x4 xw = *(const u32x4*)(p.x + (size_t)row * D_ + c0);
    u32x4 hw;
#pragma unroll
    for (int i = 0; i < 2; ++i) {
      hw[i] = pack2(rbf(s0[2 * i]) + bf2f((u16)(xw[i] & 0xffff)), rbf(s0[2 * i + 1]) + bf2f((u16)(xw[i] >> 16)));
      hw[2 + i] = pack2(rbf(s1[2 * i]) + bf2f((u16)(xw[2 + i] & 0xffff)), rbf(s1[2 * i + 1]) + bf2f((u16)(xw[2 + i] >> 16)));
    }
#pragma unroll
    for (int i = 0; i < 4; ++i) { v[hf * 8 + 2 * i] = bf2f((u16)(hw[i] & 0xffff)); v[hf * 8 + 2 * i + 1] = bf2f((u16)(hw[i] >> 16)); }
    if (hf == 0 || two) st_sc1_16(rW, WS_H + (u32)(row * D_ + c0) * 2, hw);
  }
  float sum = 0.f;
#pragma unroll
  for (int i = 0; i < 8; ++i) sum += v[i] + (two ? v[8 + i] : 0.f);
#pragma unroll
  for (int m = 32; m >= 1; m >>= 1) sum += __shfl_xor(sum, m);
  float mean = sum * (1.0f / D_), sq = 0.f;
#pragma unroll
  for (int i = 0; i < 8; ++i) { float d = v[i] - mean, e = v[8 + i] - mean; sq += d * d + (two ? e * e : 0.f); }
#pragma unroll
  for (int m = 32; m >= 1; m >>= 1) sq += __shfl_xor(sq, m);
  float mbf = rbf(mean), den = rbf(sqrtf(rbf(rbf(sq * (1.0f / D_)) + 1e-5f)));
#pragma unroll
  for (int hf = 0; hf < 2; ++hf) {
    u32x4 o;
#pragma unroll
    for (int i = 0; i < 4; ++i) o[i] = pack2(rbf(v[hf * 8 + 2 * i] - mbf) / den, rbf(v[hf * 8 + 2 * i + 1] - mbf) / den);
    if (hf == 0 || two) st_sc1_16(rW, WS_HN + (u32)(row * D_ + (hf == 0 ? lane : 64 + lane) * 8) * 2, o);
  }
}

template <bool FINE>
__device__ __forceinline__ void phase_b(int mb, int gb, char* ws, rsrc_t rW, char* smem) {
  const int wave = __builtin_amdgcn_readfirstlane(threadIdx.x >> 6);
  if (FINE || gb < 32) {
    const int hh = gb & 31;
    wait_flag(ws, WS_FQKV + (u32)(mb * 96 + 64 + hh) * 64, 1);
#pragma unroll 1
    for (int j = 0; j < 4; ++j) wait_flag(ws, WS_FCH + (u32)(hh * 4 + j) * 64, (u32)(mb + 1));
    acquire_bar();
    if (wave < (FINE ? 2 : 8)) attn_wave(ws, rW, mb * 8 + (FINE ? (gb >> 5) * 2 : 0) + wave, hh, (u16*)(smem + wave * 19200));
    signal_flag(ws, WS_FAT + (u32)mb * 64);
  }
  if (FINE) {
    if (gb < 120) {
      int mi = gb & 1, nt = (gb >> 1) % 5, ks = gb / 10;
      wait_flag(ws, WS_FAT + (u32)mb * 64, 128);
      acquire_bar();
      const FP p = getp();
      gtile<2, 2, 2, E_SLAB>((const u16*)(ws + WS_OX) + ks * 512, HE_, p.Wo + (size_t)ks * 512 * D_, nullptr, D_, 8, mb * 256 + mi * 128, nt * 128, rW,
                             WS_SLAB + (u32)ks * (S_ * D_ * 4), D_, nullptr, nullptr, smem);
      signal_flag(ws, WS_FOP + (u32)(mb * 4 + mi) * 64);
    }
  } else if (gb >= 32 && gb < 72) {
    int t = gb - 32, nt = t % 5, ks = t / 5;
    wait_flag(ws, WS_FAT + (u32)mb * 64, 32);
    acquire_bar();
    const FP p = getp();
    gtile<4, 2, 1, E_SLAB>((const u16*)(ws + WS_OX) + ks * 768, HE_, p.Wo + (size_t)ks * 768 * D_, nullptr, D_, 12, mb * 256, nt * 128, rW,
                           WS_SLAB + (u32)ks * (S_ * D_ * 4), D_, nullptr, nullptr, smem);
    signal_flag(ws, WS_FOP + (u32)(mb * 4) * 64);
  }
  if (FINE || (gb >= 32 && gb < 54)) {
    wait_flag(ws, WS_FOP + (u32)(mb * 4 + (FINE ? gb >> 6 : 0)) * 64, FINE ? 60 : 40);
    acquire_bar();
    const int rr = FINE ? gb * 2 + wave : (gb - 32) * 12 + wave;
    if (FINE ? wave < 2 : rr < 256) redln_row<FINE ? 12 : 8>(ws, rW, mb * 256 + rr);
    signal_flag(ws, WS_FLN + (u32)(mb * 4 + (FINE ? gb >> 5 : 0)) * 64);
  }
  if (FINE) {
    if (gb < 64) {
      wait_flag(ws, WS_FLN + (u32)(mb * 4 + (gb & 3)) * 64, 32);
      acquire_bar();
      const FP p = getp();
      gtile<1, 3, 2, E_GLU>((const u16*)(ws + WS_HN), D_, p.wg, p.wu, F_, 10, mb * 256 + (gb & 3) * 64, (gb >> 2) * 96, rW, WS_GU, F_, nullptr, nullptr, smem);
      signal_flag(ws, WS_FGL + (u32)(mb * 4 + (gb & 3)) * 64);
    } else if (gb < 104) {
      wait_flag(ws, WS_FGL + (u32)(mb * 4 + ((gb - 64) & 3)) * 64, 16);
      acquire_bar();
      const FP p = getp();
      gtile<1, 1, 4, E_DOWN>((const u16*)(ws + WS_GU), F_, p.wd, nullptr, D_, 24, mb * 256 + ((gb - 64) & 3) * 64, ((gb - 64) >> 2) * 64, rW, 0, D_, (const u16*)(ws + WS_H), p.out, smem);
      __syncthreads();
    }
  } else {
    if (gb >= 72 && gb < 88) {
      wait_flag(ws, WS_FLN + (u32)(mb * 4) * 64, 22);
      acquire_bar();
      const FP p = getp();
      gtile<4, 3, 1, E_GLU>((const u16*)(ws + WS_HN), D_, p.wg, p.wu, F_, 10, mb * 256, (gb - 72) * 96, rW, WS_GU, F_, nullptr, nullptr, smem);
      signal_flag(ws, WS_FGL + (u32)(mb * 4) * 64);
    } else if (gb >= 88) {
      wait_flag(ws, WS_FGL + (u32)(mb * 4) * 64, 16);
      acquire_bar();
      const FP p = getp();
      gtile<1, 1, 4, E_DOWN>((const u16*)(ws + WS_GU), F_, p.wd, nullptr, D_, 24, mb * 256 + ((gb - 88) & 3) * 64, ((gb - 88) >> 2) * 64, rW, 0, D_, (const u16*)(ws + WS_H), p.out, smem);
      __syncthreads();
    }
  }
}

__device__ __forceinline__ void gemm_role(int gb, char* smem) {
  char* ws = getp().ws;
  rsrc_t rW = __builtin_amdgcn_make_buffer_rsrc(ws, 0, WS_END, 0x00020000);
  {
    int n = gb >> 1, h = n & 31;
    const FP p = getp();
    gtile<2, 3, 2, E_BF16>((const u16*)(ws + WS_XN), D_, n < 32 ? p.Wk : p.Wv, nullptr, HE_, 10, (gb & 1) * 128, h * E_, rW, n < 32 ? WS_KX : WS_VX, HE_, nullptr, nullptr, smem);
    signal_flag(ws, WS_FQKV + (u32)n * 64);
  }
#pragma unroll 1
  for (int a = gb + 64; a < 768; a += 128) {
    int mb = a / 96, n = a % 96; int mat = n >> 5, h = n & 31;
    const FP p = getp();
    const u16* W = mat == 0 ? p.Wk : (mat == 1 ? p.Wv : p.Wq);
    u32 coff = mat == 0 ? WS_KX : (mat == 1 ? WS_VX : WS_QX);
    gtile<4, 3, 1, E_BF16>((const u16*)(ws + WS_XN), D_, W, W, HE_, 10, mb * 256, h * E_, rW, coff, HE_, nullptr, nullptr, smem);
    signal_flag(ws, WS_FQKV + (u32)(mb * 96 + n) * 64);
  }
#pragma unroll 1
  for (int mb = 0; mb < 7; ++mb) {
    phase_b<false>(mb, gb, ws, rW, smem);
  }
}

constexpr int MST = 656, QST = 80, VST = 208, PST = 72;
constexpr int CL_MEM = 0;
constexpr int CL_Q   = 8 * MST * 2;
constexpr int KBUF   = 48 * QST * 2;
constexpr int VBUF   = 40 * VST * 2;
constexpr int CL_K   = CL_Q + 8 * QST * 2;
constexpr int CL_V   = CL_K + 2 * KBUF;
constexpr int CL_P   = CL_V + 2 * VBUF;
constexpr int CL_F   = CL_P + 8 * PST * 2;
constexpr int CL_END = CL_F + 16;
constexpr int CL_STG = CL_Q;

constexpr int DLY_A2I = 0;
constexpr int DLY_S = 8;
constexpr int DLY_B = 4;
constexpr int DLY_V = 3;
constexpr int DLY_A1 = 0;
constexpr int DLY_A2 = 0;
constexpr int NPOLL = 11;

__device__ __forceinline__ void chain_role(int cu, char* smem) {
  const FP p = getp();
  const int h = cu >> 2, j = cu & 3;
  const int tid = threadIdx.x, lane = tid & 63;
  const int wave = __builtin_amdgcn_readfirstlane(tid >> 6);
  const int fr = lane & 15, g = lane >> 4, q4 = fr >> 2, p4 = fr & 3;
  char* ws = p.ws;
  u32* ctl = (u32*)(ws + WS_CTL);
  rsrc_t rW = __builtin_amdgcn_make_buffer_rsrc(ws, 0, WS_END, 0x00020000);
  const u32 a2base0 = WS_GA2 + (u32)(cu % A2_REP) * A2_STRIDE;

  bf16x8 Wf[20];
  bf16x8 Wof[6];
  {
#pragma unroll 1
    for (int rnd = 0; rnd < 2; ++rnd) {
      if (rnd) __syncthreads();
#pragma unroll 1
      for (int ii = wave; ii < 120; ii += 12) {
        int m2 = ii / 60, c = (ii % 60) * 64 + lane;
        const u16* sp;
        if (rnd == 1 && m2 == 1) { int e = c / 20, ch = c % 20; sp = p.Wo + (size_t)(h * E_ + e) * D_ + j * 160 + ch * 8; }
        else { const u16* W = rnd == 0 ? (m2 == 0 ? p.Wq : p.Wk) : p.Wv; int d = c / 6, ch = c % 6; sp = W + (size_t)d * HE_ + h * E_ + j * 48 + ch * 8; }
        __builtin_amdgcn_global_load_lds((const u32*)sp, (LAS u32*)(smem + ii * 1024), 16, 0, 0);
      }
      asm volatile("s_waitcnt vmcnt(0)" ::: "memory");
      __syncthreads();
      const int mat = wave / 3;
      if ((rnd == 0 && mat < 2) || (rnd == 1 && mat == 2)) {
        const u16* stg = (const u16*)(smem + (rnd == 0 ? mat : 0) * 61440);
        int e0 = (wave % 3) * 16;
#pragma unroll
        for (int ks = 0; ks < 20; ++ks) {
          int r0 = ks * 32 + g * 8 + q4;
          Wf[ks] = cat8(tr_read(stg + r0 * 48 + e0 + p4 * 4), tr_read(stg + (r0 + 4) * 48 + e0 + p4 * 4));
        }
      }
    }
    const u16* stg = (const u16*)(smem + 61440);
    const int wc = wave < 10 ? wave : 0;
#pragma unroll
    for (int t = 0; t < 6; ++t) {
      int r0 = t * 32 + g * 8 + q4;
      Wof[t] = cat8(tr_read(stg + r0 * 160 + wc * 16 + p4 * 4), tr_read(stg + (r0 + 4) * 160 + wc * 16 + p4 * 4));
    }
    __syncthreads();
  }
  for (int i = tid; i < (CL_END - CL_Q) / 4; i += NTH) ((u32*)(smem + CL_Q))[i] = 0u;
  __syncthreads();

  u16* memL = (u16*)(smem + CL_MEM);
  u16* qL = (u16*)(smem + CL_Q);
  u16* pL = (u16*)(smem + CL_P);
  volatile LAS u32* vflag = (volatile LAS u32*)(smem + CL_F);
  volatile LAS u32* pflag = vflag + 1;
  volatile LAS u32* rflag = vflag + 2;
  bool dead = false;

  auto xfetch = [&](int seg, int bf, int ii, int ln) {
    u32 off;
    if (ii < 5) { int c = ii * 64 + ln; int tok = c / 10, ch = c % 10;
      off = ch < 6 ? WS_KX + (u32)((seg * SEG_ + tok) * HE_ + h * E_ + j * 48 + ch * 8) * 2 : 2048u;
      __builtin_amdgcn_global_load_lds((const u32*)(ws + off), (LAS u32*)(smem + CL_K + bf * KBUF + 8 * QST * 2 + ii * 1024), 16, 0, 16);
    } else { int c = (ii - 5) * 64 + ln; int tok = c / 26, ch = c % 26; if (ch >= 24) ch = 0;
      off = WS_VX + (u32)((seg * SEG_ + tok) * HE_ + h * E_ + ch * 8) * 2;
      __builtin_amdgcn_global_load_lds((const u32*)(ws + off), (LAS u32*)(smem + CL_V + bf * VBUF + 8 * VST * 2 + (ii - 5) * 1024), 16, 0, 16);
    }
  };
  auto wait_kv = [&](int mb) {
    u32* fk = (u32*)(ws + WS_FQKV + (u32)(mb * 96 + h) * 64); u32* fv = (u32*)(ws + WS_FQKV + (u32)(mb * 96 + 32 + h) * 64);
    u32 spins = 0;
    for (;;) {
      u32 a = __hip_atomic_load(fk, RLX_AGENT), b = __hip_atomic_load(fv, RLX_AGENT);
      const u32 need = mb == 0 ? 2u : 1u;
      if (__builtin_amdgcn_readfirstlane(a) >= need && __builtin_amdgcn_readfirstlane(b) >= need) break;
      __builtin_amdgcn_s_sleep(4);
      if (dead || spin_fail(spins, ctl)) { dead = true; break; }
    }
    asm volatile("" ::: "memory");
  };
  wait_kv(0);
  for (int ii = wave; ii < 18; ii += 12) xfetch(0, 0, ii, lane);
  asm volatile("s_waitcnt vmcnt(0)" ::: "memory");
  __syncthreads();
  u32 vwx[4];
  auto calc_vwx = [&](int bf, int fr_, int g_) {
    const u16* vb = (const u16*)(smem + CL_V + bf * VBUF) + (8 + fr_) * VST + g_ * 8;
    f32x4 a0 = {0.f, 0.f, 0.f, 0.f}, a1 = {0.f, 0.f, 0.f, 0.f};
#pragma unroll
    for (int t = 0; t < 6; ++t) {
      a0 = mfma16(*(const bf16x8*)(vb + t * 32), Wof[t], a0);
      a1 = mfma16(*(const bf16x8*)(vb + 16 * VST + t * 32), Wof[t], a1);
    }
    vwx[0] = pack2(a0[0], a0[1]); vwx[1] = pack2(a0[2], a0[3]); vwx[2] = pack2(a1[0], a1[1]); vwx[3] = pack2(a1[2], a1[3]);
  };
  calc_vwx(0, fr, g);

#pragma unroll 1
  for (int s = 0; s < NSEG_; ++s) {
    int tz = threadIdx.x; asm volatile("" : "+v"(tz));
    const int tid = tz, lane = tz & 63, fr = lane & 15, g = lane >> 4;
    const u32 ep = (u32)s + 1u;
    const u32 par = ((u32)(s / RNG) + 1u) & 1u;
    const u32 WS_GB = WS_GB0 + (u32)(s % RNG) * GB_SZ, WS_GA1 = WS_GA10 + (u32)(s % RNG) * A1_SZ;
    const u32 a2w = (u32)(s % RNG) * A2_SZ, a2base = a2base0 + (u32)((s + RNG - 1) % RNG) * A2_SZ;
    const int buf = s & 1;
    u16* kL = (u16*)(smem + CL_K + buf * KBUF);
    u16* vL = (u16*)(smem + CL_V + buf * VBUF);
    if (s == 0) {
      for (int c = tid; c < 8 * 80; c += NTH) { int r = c / 80, ch = c % 80; *(u32x4*)(memL + r * MST + ch * 8) = *(const u32x4*)(getp().mem0 + r * D_ + ch * 8); }
    } else if (wave < 10) {
      const u32 epm = (u32)s;
      u32x4 a0, a1; u32 spins = 0;
      for (int i = 0; i < DLY_A2I; ++i) __builtin_amdgcn_s_sleep(4);
      for (;;) {
        asm volatile("" ::: "memory");
        a0 = ld_sc1_16(rW, a2base + tid * 16); a1 = ld_sc1_16(rW, a2base + (tid + 640) * 16);
        bool ok = a0[1] == epm && a0[3] == epm && a1[1] == epm && a1[3] == epm;
        if (__all(ok)) break;
        for (int i = 0; i < DLY_A2; ++i) __builtin_amdgcn_s_sleep(2);
        if (dead || spin_fail(spins, ctl)) { dead = true; break; }
      }
      { int u = tid; int row = ((u >> 4) & 1) * 4 + ((u >> 2) & 3), col = (u >> 5) * 16 + (u & 3) * 4; *(u32x2*)(memL + row * MST + col) = (u32x2){a0[0], a0[2]}; }
      { int u = tid + 640; int row = ((u >> 4) & 1) * 4 + ((u >> 2) & 3), col = (u >> 5) * 16 + (u & 3) * 4; *(u32x2*)(memL + row * MST + col) = (u32x2){a1[0], a1[2]}; }
    }
    lbar();
    if (wave < 9) {
      f32x4 acc = {0.f, 0.f, 0.f, 0.f};
      const u16* mb = memL + (fr & 7) * MST + g * 8;
      bf16x8 b0[4], b1[4];
#pragma unroll
      for (int i = 0; i < 4; ++i) b0[i] = *(const bf16x8*)(mb + i * 32);
#pragma unroll
      for (int gq = 0; gq < 5; ++gq) {
        if (gq & 1) {
          if (gq < 4) {
#pragma unroll
            for (int i = 0; i < 4; ++i) b0[i] = *(const bf16x8*)(mb + ((gq + 1) * 4 + i) * 32);
          }
#pragma unroll
          for (int i = 0; i < 4; ++i) acc = mfma16(Wf[gq * 4 + i], b1[i], acc);
        } else {
          if (gq < 4) {
#pragma unroll
            for (int i = 0; i < 4; ++i) b1[i] = *(const bf16x8*)(mb + ((gq + 1) * 4 + i) * 32);
          }
#pragma unroll
          for (int i = 0; i < 4; ++i) acc = mfma16(Wf[gq * 4 + i], b0[i], acc);
        }
        __builtin_amdgcn_sched_barrier(0);
      }
      u32x2 w = {pack2(acc[0], acc[1]), pack2(acc[2], acc[3])};
      int e = (wave % 3) * 16 + g * 4;
      if (fr < 8) {
        if (wave < 3) *(u32x2*)(qL + fr * QST + e) = w;
        else if (wave < 6) {
          *(u32x2*)(kL + fr * QST + e) = w;
          st_sc1_8(rW, WS_KM + (u32)((s * M_ + fr) * HE_ + h * E_ + j * 48 + e) * 2, w);
        } else {
          st_sc1_16(rW, WS_GB + (u32)(cu * 192 + 96 + (wave - 6) * 32 + fr * 4 + g) * 16, (u32x4){w[0], ep, w[1], ep});
          st_sc1_8(rW, WS_VM + (u32)((s * M_ + fr) * HE_ + h * E_ + j * 48 + e) * 2, w);
        }
      }
    }
    if (s == NSEG_ - 1) break;
    lbar();
    if (wave == 10) {
      if (((s + 1) & 7) == 0) wait_kv((s + 1) >> 3);
#pragma unroll 1
      for (int ii = 0; ii < 18; ++ii) xfetch(s + 1, buf ^ 1, ii, lane);
    }

    if (wave < 3) {
      f32x4 acc = {0.f, 0.f, 0.f, 0.f};
#pragma unroll
      for (int kk = 0; kk < 2; ++kk) {
        bf16x8 a = *(const bf16x8*)(kL + (wave * 16 + fr) * QST + kk * 32 + g * 8);
        bf16x8 b = *(const bf16x8*)(qL + (fr & 7) * QST + kk * 32 + g * 8);
        acc = mfma16(a, b, acc);
      }
      if (fr < 8)
        st_sc1_16(rW, WS_GB + (u32)(cu * 192 + wave * 32 + fr * 4 + g) * 16,
                  (u32x4){(__float_as_uint(acc[0]) & ~1u) | par, (__float_as_uint(acc[1]) & ~1u) | par,
                          (__float_as_uint(acc[2]) & ~1u) | par, (__float_as_uint(acc[3]) & ~1u) | par});
    }
    if (wave >= NPOLL) {
      const int r = lane & 7, q = lane >> 3;
      bool got = true;
      for (int i = 0; i < DLY_B; ++i) __builtin_amdgcn_s_sleep(4);
      const u32 o0 = WS_GB + (u32)(h * 4 * 192 + (q >> 2) * 32 + r * 4 + (q & 3)) * 16;
      const u32 o1 = WS_GB + (u32)(h * 4 * 192 + 64 + r * 4 + (q & 1)) * 16;
      u32x4 a[4], b[4]; u32 spins = 0;
      for (;;) {
        asm volatile("" ::: "memory");
        u32 aa = 0xffffffffu, oo = 0u;
#pragma unroll
        for (int i = 0; i < 4; ++i) {
          a[i] = __builtin_amdgcn_raw_buffer_load_b128(rW, o0, i * 3072, 16);
          b[i] = __builtin_amdgcn_raw_buffer_load_b128(rW, o1, i * 3072, 16);
        }
#pragma unroll
        for (int i = 0; i < 4; ++i) {
          aa &= a[i][0] & a[i][1] & a[i][2] & a[i][3] & b[i][0] & b[i][1] & b[i][2] & b[i][3];
          oo |= a[i][0] | a[i][1] | a[i][2] | a[i][3] | b[i][0] | b[i][1] | b[i][2] | b[i][3];
        }
        bool ok = (aa & 1u) == par && (oo & 1u) == par;
        if (__all(ok)) break;
        if (*pflag == ep) { got = false; break; }
        if (dead || spin_fail(spins, ctl)) { dead = true; break; }
      }
      if (got) {
      const float scale = 0.07216878364870322f;
      float sc[8];
#pragma unroll
      for (int k = 0; k < 4; ++k) {
        float x = (__uint_as_float(a[0][k]) + __uint_as_float(a[1][k])) + (__uint_as_float(a[2][k]) + __uint_as_float(a[3][k]));
        float y = (__uint_as_float(b[0][k]) + __uint_as_float(b[1][k])) + (__uint_as_float(b[2][k]) + __uint_as_float(b[3][k]));
        sc[k] = rbf(rbf(x) * scale); sc[4 + k] = rbf(rbf(y) * scale);
      }
      float mx = fmaxf(fmaxf(fmaxf(sc[0], sc[1]), fmaxf(sc[2], sc[3])), fmaxf(fmaxf(sc[4], sc[5]), fmaxf(sc[6], sc[7])));
      mx = fmaxf(mx, __uint_as_float(__builtin_amdgcn_update_dpp(0, __float_as_uint(mx), 0x128, 0xf, 0xf, false)));
      mx = red16_max(mx); mx = red32_max(mx);
      float sum = 0.f, sum2 = 0.f;
#pragma unroll
      for (int k = 0; k < 4; ++k) { sc[k] = __expf(sc[k] - mx); sum += sc[k]; sc[4 + k] = __expf(sc[4 + k] - mx); sum2 += sc[4 + k]; }
      if (q < 2) sum += sum2;
      sum += __uint_as_float(__builtin_amdgcn_update_dpp(0, __float_as_uint(sum), 0x128, 0xf, 0xf, false));
      sum = red16_add(sum); sum = red32_add(sum);
      float inv = __builtin_amdgcn_rcpf(sum);
      *(u32x2*)(pL + r * PST + q * 4) = (u32x2){pack2(sc[0] * inv, sc[1] * inv), pack2(sc[2] * inv, sc[3] * inv)};
      if (q < 2) *(u32x2*)(pL + r * PST + 32 + q * 4) = (u32x2){pack2(sc[4] * inv, sc[5] * inv), pack2(sc[6] * inv, sc[7] * inv)};
      asm volatile("s_waitcnt lgkmcnt(0)" ::: "memory");
      if (lane == 0) *pflag = ep;
      }
    } else if (wave == 9) {
      u32x4 v[6]; u32 spins = 0;
      for (int i = 0; i < DLY_V; ++i) __builtin_amdgcn_s_sleep(4);
      for (;;) {
        asm volatile("" ::: "memory");
        bool ok = true;
#pragma unroll
        for (int i = 0; i < 6; ++i) { int id = lane + 64 * i; int src = id / 96, u = id % 96; v[i] = ld_sc1_16(rW, WS_GB + (u32)((h * 4 + src) * 192 + 96 + u) * 16); }
#pragma unroll
        for (int i = 0; i < 6; ++i) ok = ok && v[i][1] == ep && v[i][3] == ep;
        if (__all(ok)) break;
        if (dead || spin_fail(spins, ctl)) { dead = true; break; }
      }
#pragma unroll
      for (int i = 0; i < 6; ++i) {
        int id = lane + 64 * i; int src = id / 96, u = id % 96; int vt = u >> 5, rr = (u >> 2) & 7, gg = u & 3;
        *(u32x2*)(vL + rr * VST + src * 48 + vt * 16 + gg * 4) = (u32x2){v[i][0], v[i][2]};
      }
      asm volatile("s_waitcnt lgkmcnt(0)" ::: "memory");
      if (lane == 0) *vflag = ep;
    }
    if (wave < 10) {
      while (*vflag != ep) { if (dead) break; }
      asm volatile("" ::: "memory");
      f32x4 am = {0.f, 0.f, 0.f, 0.f};
      const u16* vb = vL + (fr & 7) * VST + g * 8;
#pragma unroll
      for (int t = 0; t < 6; ++t) am = mfma16(*(const bf16x8*)(vb + t * 32), Wof[t], am);
      u32x4 a0u = {vwx[0], vwx[1], vwx[2], vwx[3]};
      u32x4 a1u = {g < 2 ? pack2(am[0], am[1]) : 0u, g < 2 ? pack2(am[2], am[3]) : 0u, 0u, 0u};
      while (*pflag != ep) { if (dead) break; }
      asm volatile("s_waitcnt vmcnt(0)" ::: "memory");
      const u16* pr = pL + (fr & 7) * PST;
      u32x2 x0 = *(const u32x2*)(pr + 8 + g * 4), x1 = *(const u32x2*)(pr + 24 + g * 4), x2 = *(const u32x2*)(pr + (g & 1) * 4);
      u32x4 b0u = {x0[0], x0[1], x1[0], x1[1]};
      u32x4 b1u = {g < 2 ? x2[0] : 0u, g < 2 ? x2[1] : 0u, 0u, 0u};
      f32x4 acc = {0.f, 0.f, 0.f, 0.f};
      acc = mfma16(__builtin_bit_cast(bf16x8, a0u), __builtin_bit_cast(bf16x8, b0u), acc);
      acc = mfma16(__builtin_bit_cast(bf16x8, a1u), __builtin_bit_cast(bf16x8, b1u), acc);
      if (fr < 8) {
        u32 o = WS_GA1 + (u32)(((j * 10 + wave) * 32 + h) * 32 + fr * 4 + g) * 16;
        st_sc1_16(rW, o, (u32x4){(__float_as_uint(acc[0]) & ~1u) | par, (__float_as_uint(acc[1]) & ~1u) | par,
                                 (__float_as_uint(acc[2]) & ~1u) | par, (__float_as_uint(acc[3]) & ~1u) | par});
      }
    }
    if (wave == 10) asm volatile("s_waitcnt vmcnt(0)" ::: "memory");
    lbar();
    if ((s & 7) == 7 && tid == 0) __hip_atomic_store((u32*)(ws + WS_FCH + (u32)cu * 64), (u32)(s >> 3) + 1u, RLX_AGENT);
    if (wave < 10) calc_vwx(buf ^ 1, fr, g);
    if (wave >= NPOLL) {
      if (cu < 80) {
        const int ch = lane & 15, hq = lane >> 4;
        bool got = true;
        for (int i = 0; i < DLY_A1; ++i) __builtin_amdgcn_s_sleep(4);
        const u32 rbase = WS_GA1 + (u32)(((cu >> 1) * 32 + hq * 8) * 32 + (cu & 1) * 16 + ch) * 16;
        u32x4 a[8]; u32 spins = 0;
        for (;;) {
          asm volatile("" ::: "memory");
          u32 aa = 0xffffffffu, oo = 0u;
#pragma unroll
          for (int i = 0; i < 8; ++i) a[i] = __builtin_amdgcn_raw_buffer_load_b128(rW, rbase, i * 512, 16);
#pragma unroll
          for (int i = 0; i < 8; ++i) { aa &= a[i][0] & a[i][1] & a[i][2] & a[i][3]; oo |= a[i][0] | a[i][1] | a[i][2] | a[i][3]; }
          bool ok = (aa & 1u) == par && (oo & 1u) == par;
          if (__all(ok)) break;
          if (*rflag == ep) { got = false; break; }
          if (dead || spin_fail(spins, ctl)) { dead = true; break; }
        }
        if (got) {
        float t[4];
#pragma unroll
        for (int k = 0; k < 4; ++k) {
          float x = ((__uint_as_float(a[0][k]) + __uint_as_float(a[1][k])) + (__uint_as_float(a[2][k]) + __uint_as_float(a[3][k])))
                  + ((__uint_as_float(a[4][k]) + __uint_as_float(a[5][k])) + (__uint_as_float(a[6][k]) + __uint_as_float(a[7][k])));
          x = red16_add(x); x = red32_add(x); t[k] = x;
        }
        u32 w0 = pack2(t[0], t[1]), w1 = pack2(t[2], t[3]);
#pragma unroll
        for (int it = 0; it < A2_REP / 4; ++it) st_sc1_16(rW, a2w + WS_GA2 + (u32)(cu * 16 + ch) * 16 + (u32)(hq + 4 * it) * A2_STRIDE, (u32x4){w0, ep, w1, ep});
        if (lane == 0) *rflag = ep;
        }
      }
      else if (wave == 11) {
        u32 spins = 0;
        for (int i = 0; i < DLY_S; ++i) __builtin_amdgcn_s_sleep(4);
        for (;;) {
          asm volatile("" ::: "memory");
          u32x4 a = ld_sc1_16(rW, a2base0 + a2w + (u32)(cu - 80) * 256);
          if (__all(a[1] == ep)) break;
          if (dead || spin_fail(spins, ctl)) { dead = true; break; }
        }
      }
    }
    lbar();
  }
  asm volatile("s_waitcnt vmcnt(0)" ::: "memory");
  __syncthreads();
  if (tid == 0) __hip_atomic_store((u32*)(ws + WS_FCH + (u32)cu * 64), 8u, RLX_AGENT);
  phase_b<true>(7, cu, ws, rW, smem);
}

__global__ __launch_bounds__(NTH, 3) void k_main(FP p) {
  extern __shared__ __attribute__((aligned(16))) char smem[];
  const int b = blockIdx.x;
  const int id = (b >> 4) * 8 + (b & 7);
  if ((b >> 3) & 1) gemm_role(id, smem);
  else chain_role(id, smem);
}

extern "C" void kernel_launch(void* const* d_in, const int* in_sizes, int n_in,
                              void* d_out, int out_size, void* d_ws, size_t ws_size,
                              hipStream_t stream) {
  char* ws = (char*)d_ws;
  (void)hipFuncSetAttribute((const void*)k_main, hipFuncAttributeMaxDynamicSharedMemorySize, LDS_BYTES);
  k_ln<<<S_ / 4, 256, 0, stream>>>((const u16*)d_in[0], (u16*)(ws + WS_XN), (u32x4*)ws);
  FP p{(const u16*)d_in[0], (const u16*)d_in[1], (const u16*)d_in[2], (const u16*)d_in[3], (const u16*)d_in[4], (const u16*)d_in[5],
       (const u16*)d_in[8], (const u16*)d_in[9], (const u16*)d_in[10], (u16*)d_out, ws};
  k_main<<<256, NTH, LDS_BYTES, stream>>>(p);
}
```
